```python
import torch

def _softmax(x, axis=-1):
    return torch.softmax(x, dim=axis)
def _gelu(x):
    return torch.nn.functional.gelu(x, approximate="tanh")
def _silu(x):
    return torch.nn.functional.silu(x)
def _sigmoid(x):
    return torch.sigmoid(x)
def _layernorm(x, axis=-1, eps=1e-5):
    m = x.mean(dim=axis, keepdim=True)
    v = x.var(dim=axis, keepdim=True, unbiased=False)
    return (x - m) / torch.sqrt(v + eps)
def _scatter_add(tgt, idx, src, axis=0):
    return tgt.clone().index_add_(axis, idx, src)
def _segment_sum(src, seg, n):
    out = torch.zeros((n,) + src.shape[1:], dtype=src.dtype)
    return out.index_add_(0, seg, src)
def _pad(x, pw):
    flat = tuple(v for pair in reversed(pw) for v in pair)
    return torch.nn.functional.pad(x, flat)

kernel_name = 'parallel_sum__grouped_kv_mqa__glu_gated_reglu__prompt'

def setup_inputs():

    _w = (lambda a: (lambda t: t.to(torch.bfloat16) if t.is_floating_point() else t)(torch.as_tensor(a)))
    def _A_setup():
        B, S, D, H = 1, 2048, 640, 32
        Dh = D // H
        return {
            "x": _w((lambda *s: torch.randn(*s, dtype=torch.bfloat16) * 0.05)(B, S, D)),
            "wq": _w((lambda *s: torch.randn(*s, dtype=torch.bfloat16) * 0.05)(D, H * Dh)),
            "wk": _w((lambda *s: torch.randn(*s, dtype=torch.bfloat16) * 0.05)(D, Dh)),
            "wv": _w((lambda *s: torch.randn(*s, dtype=torch.bfloat16) * 0.05)(D, Dh)),
            "wo": _w((lambda *s: torch.randn(*s, dtype=torch.bfloat16) * 0.05)(H * Dh, D)),
        }
    _A_in = _A_setup()

    def _B_setup():
        B, S, D, F = 1, 2048, 640, 1728
        return {
            "x": _w((lambda *s: torch.randn(*s, dtype=torch.bfloat16) * 0.05)(B, S, D)),
            "W_gate": _w((lambda *s: torch.randn(*s, dtype=torch.bfloat16) * 0.8)(D, F) * (2.0 / D) ** 0.5),
            "W_up": _w((lambda *s: torch.randn(*s, dtype=torch.bfloat16) * 0.8)(D, F) * (2.0 / D) ** 0.5),
            "W_down": _w((lambda *s: torch.randn(*s, dtype=torch.bfloat16) * 0.8)(F, D) * (1.0 / F) ** 0.5),
        }
    _B_in = _B_setup()

    return {
        "A_x": _A_in["x"],
        "A_wq": _A_in["wq"],
        "A_wk": _A_in["wk"],
        "A_wv": _A_in["wv"],
        "A_wo": _A_in["wo"],
        "B_x": _B_in["x"],
        "B_W_gate": _B_in["W_gate"],
        "B_W_up": _B_in["W_up"],
        "B_W_down": _B_in["W_down"],
    }

def reference(A_x, A_wq, A_wk, A_wv, A_wo, B_x, B_W_gate, B_W_up, B_W_down):
    def _A_ref(x, wq, wk, wv, wo):
        B, S, D = x.shape
        H = 32
        Dh = D // H
        q = (lambda x, *d: x.permute(*d))((x @ wq).reshape(B, S, H, Dh), 0, 2, 1, 3)
        k = x @ wk
        v = x @ wv
        scores = torch.einsum("bhqd,bkd->bhqk", q, k) * (Dh ** -0.5)
        p = _softmax(scores, axis=-1)
        o = torch.einsum("bhqk,bkd->bhqd", p, v)
        return (lambda x, *d: x.permute(*d))(o, 0, 2, 1, 3).reshape(B, S, H * Dh) @ wo

    def _B_ref(x, W_gate, W_up, W_down):
        gate = torch.einsum("bsd,df->bsf", x, W_gate)
        up = torch.einsum("bsd,df->bsf", x, W_up)
        gate = gate * (lambda x, dt: x.to(dt))(gate > 0, gate.dtype)
        h = gate * up
        return torch.einsum("bsf,fd->bsd", h, W_down)

    x = A_x
    xn = _layernorm(x, axis=-1)
    return x + _A_ref(xn, A_wq, A_wk, A_wv, A_wo) + _B_ref(xn, B_W_gate, B_W_up, B_W_down)
```

```cpp
#include <hip/hip_runtime.h>
#include <hip/hip_bf16.h>
typedef unsigned short u16;
__device__ __forceinline__ float bf2f(u16 v){ return __uint_as_float(((unsigned)v)<<16); }
__device__ __forceinline__ u16 f2bf(float f){ __hip_bfloat16 b = __float2bfloat16(f); return *reinterpret_cast<u16*>(&b); }
__device__ __forceinline__ float rbf(float f){ return bf2f(f2bf(f)); }
constexpr int S=2048,D=640,H=32,DH=20,F=1728;
__global__ void k_ln(const u16* x, u16* xn){
  int row=blockIdx.x; __shared__ float red[64];
  float s=0; for(int i=threadIdx.x;i<D;i+=64) s+=bf2f(x[row*D+i]);
  for(int o=32;o;o>>=1) s+=__shfl_xor(s,o);
  float mean=s/D; float q=0;
  for(int i=threadIdx.x;i<D;i+=64){ float d=bf2f(x[row*D+i])-mean; q+=d*d; }
  for(int o=32;o;o>>=1) q+=__shfl_xor(q,o);
  float m=rbf(mean), v=rbf(q/D); float sd=rbf(sqrtf(rbf(v+1e-5f)));
  for(int i=threadIdx.x;i<D;i+=64) xn[row*D+i]=f2bf(rbf(bf2f(x[row*D+i])-m)/sd);
}
__global__ void k_gemm(const u16* A,const u16* B,u16* C,int M,int N,int K){
  int n=blockIdx.x*64+threadIdx.x, m=blockIdx.y; if(n>=N) return;
  float acc=0; for(int k=0;k<K;k++) acc+=bf2f(A[m*K+k])*bf2f(B[k*N+n]);
  C[m*N+n]=f2bf(acc);
}
__global__ void k_attn(const u16* q,const u16* k,const u16* v,u16* o){
  int idx=blockIdx.x*64+threadIdx.x; int t=idx/H, h=idx%H;
  float qq[DH]; for(int d=0;d<DH;d++) qq[d]=bf2f(q[t*D+h*DH+d]);
  float m=-1e30f,l=0,acc[DH]; for(int d=0;d<DH;d++) acc[d]=0;
  for(int j=0;j<S;j++){ float s=0; for(int d=0;d<DH;d++) s+=qq[d]*bf2f(k[j*DH+d]); s*=0.22360679775f;
    float mn=fmaxf(m,s); float a=__expf(m-mn), p=__expf(s-mn); l=l*a+p; for(int d=0;d<DH;d++) acc[d]=acc[d]*a+p*bf2f(v[j*DH+d]); m=mn; }
  for(int d=0;d<DH;d++) o[t*D+h*DH+d]=f2bf(acc[d]/l);
}
__global__ void k_glu(const u16* g,const u16* u,u16* h){ int i=blockIdx.x*256+threadIdx.x; float gg=bf2f(g[i]); h[i]=f2bf((gg>0?gg:0.f)*bf2f(u[i])); }
__global__ void k_fin(const u16* x,const u16* a,const u16* b,u16* out){ int i=blockIdx.x*256+threadIdx.x; out[i]=f2bf(rbf(bf2f(x[i])+bf2f(a[i]))+bf2f(b[i])); }
extern "C" void kernel_launch(void* const* d_in, const int* in_sizes, int n_in, void* d_out, int out_size, void* d_ws, size_t ws_size, hipStream_t stream) {
  const u16 *x=(const u16*)d_in[0],*wq=(const u16*)d_in[1],*wk=(const u16*)d_in[2],*wv=(const u16*)d_in[3],*wo=(const u16*)d_in[4];
  const u16 *wg=(const u16*)d_in[6],*wu=(const u16*)d_in[7],*wd=(const u16*)d_in[8];
  u16* ws=(u16*)d_ws; u16* xn=ws; u16* q=xn+S*D; u16* k=q+S*D; u16* v=k+S*DH; u16* o=v+S*DH; u16* g=o+S*D; u16* u=g+S*F; u16* h=u+S*F; u16* a=h+S*F; u16* b=a+S*D;
  k_ln<<<S,64,0,stream>>>(x,xn);
  k_gemm<<<dim3(10,S),64,0,stream>>>(xn,wq,q,S,D,D);
  k_gemm<<<dim3(1,S),64,0,stream>>>(xn,wk,k,S,DH,D);
  k_gemm<<<dim3(1,S),64,0,stream>>>(xn,wv,v,S,DH,D);
  k_gemm<<<dim3(27,S),64,0,stream>>>(xn,wg,g,S,F,D);
  k_gemm<<<dim3(27,S),64,0,stream>>>(xn,wu,u,S,F,D);
  k_attn<<<S*H/64,64,0,stream>>>(q,k,v,o);
  k_glu<<<S*F/256,256,0,stream>>>(g,u,h);
  k_gemm<<<dim3(10,S),64,0,stream>>>(o,wo,a,S,D,D);
  k_gemm<<<dim3(10,S),64,0,stream>>>(h,wd,b,S,D,F);
  k_fin<<<S*D/256,256,0,stream>>>(x,a,b,(u16*)d_out);
}
```

```cpp
#include <hip/hip_runtime.h>
#include <hip/hip_bf16.h>
#include <utility>

typedef unsigned short u16;
typedef unsigned int u32;
typedef unsigned long long u64;
using f32x4 = __attribute__((ext_vector_type(4))) float;
using f32x16 = __attribute__((ext_vector_type(16))) float;
using f32x32 = __attribute__((ext_vector_type(32))) float;
using u32x2 = __attribute__((ext_vector_type(2))) u32;
using u32x4 = __attribute__((ext_vector_type(4))) u32;
using u32x16 = __attribute__((ext_vector_type(16))) u32;
using u32x32 = __attribute__((ext_vector_type(32))) u32;
using bf16x2 = __attribute__((ext_vector_type(2))) __bf16;
using bf16x8 = __attribute__((ext_vector_type(8))) __bf16;
using s16x4 = __attribute__((ext_vector_type(4))) short;
using f32x2 = __attribute__((ext_vector_type(2))) float;
typedef __amdgpu_buffer_rsrc_t rsrc_t;
#define DI __device__ __forceinline__
#define LAS __attribute__((address_space(3)))

constexpr int S_ = 2048, D_ = 640, F_ = 1728;
constexpr u32 WS_EPOCH = 0;
constexpr u32 WS_FLAGA = 4096, WS_FLAGB = 8192, WS_FLAGC = 12288;
constexpr u32 WS_KMAX = 16384;
constexpr u32 WS_STAT = 20480;
constexpr u32 WS_CS = WS_STAT + 32768;
constexpr u32 WS_KV = WS_CS + 65 * 2048 + 2048;
constexpr u32 WS_Q = WS_KV + 65 * 5120 + 3072;
constexpr u32 WS_ACT = WS_Q + 64 * 80 * 512;
constexpr u32 WS_WT = WS_ACT + 128 * 296 * 256;
constexpr u32 WS_DBG = WS_WT + 40 * 296 * 256;
constexpr u32 WS_END = WS_DBG + 262144;
static_assert(WS_KV % 1024 == 0 && WS_Q % 1024 == 0 && WS_ACT % 1024 == 0 && WS_WT % 1024 == 0, "align");
constexpr u32 L_RING = 0, L_WKV = 131072, L_SCR = L_WKV + 25600, L_TOTAL = 163840;
constexpr int AUX_SC1 = 16;
constexpr u32 SPIN_CAP = 1u << 14;

template <int N> struct IC { static constexpr int value = N; };
template <typename Fn, int... I> DI void sfor_impl(Fn&& f, std::integer_sequence<int, I...>) { (f(IC<I>{}), ...); }
template <int N, typename Fn> DI void sfor(Fn&& f) { sfor_impl(f, std::make_integer_sequence<int, N>{}); }

DI rsrc_t mkrs(const void* p, u32 bytes) { return __builtin_amdgcn_make_buffer_rsrc((void*)p, (short)0, (int)bytes, 0x00020000); }
DI float bflo(u32 v) { return __uint_as_float(v << 16); }
DI float bfhi(u32 v) { return __uint_as_float(v & 0xffff0000u); }
DI u32 pk2(float a, float b) { f32x2 v = {a, b}; return __builtin_bit_cast(u32, __builtin_convertvector(v, bf16x2)); }
DI float rbf(float a) { return bflo(pk2(a, 0.f)); }
template <int C> DI float dpp_add(float v) { return v + __uint_as_float(__builtin_amdgcn_update_dpp(0u, __float_as_uint(v), C, 0xf, 0xf, true)); }
template <int C> DI float dpp_max(float v) { return __builtin_fmaxf(v, __uint_as_float(__builtin_amdgcn_update_dpp(0u, __float_as_uint(v), C, 0xf, 0xf, true))); }
DI float x16_add(float v) { auto r = __builtin_amdgcn_permlane16_swap(__float_as_uint(v), __float_as_uint(v), false, false); return __uint_as_float(r[0]) + __uint_as_float(r[1]); }
DI float x32_add(float v) { auto r = __builtin_amdgcn_permlane32_swap(__float_as_uint(v), __float_as_uint(v), false, false); return __uint_as_float(r[0]) + __uint_as_float(r[1]); }
DI float x16_max(float v) { auto r = __builtin_amdgcn_permlane16_swap(__float_as_uint(v), __float_as_uint(v), false, false); return __builtin_fmaxf(__uint_as_float(r[0]), __uint_as_float(r[1])); }
DI float x32_max(float v) { auto r = __builtin_amdgcn_permlane32_swap(__float_as_uint(v), __float_as_uint(v), false, false); return __builtin_fmaxf(__uint_as_float(r[0]), __uint_as_float(r[1])); }
DI float wave_sum(float v) { v = dpp_add<0x128>(v); v = dpp_add<0x124>(v); v = dpp_add<0x122>(v); v = dpp_add<0x121>(v); return x32_add(x16_add(v)); }
DI float wave_max(float v) { v = dpp_max<0x128>(v); v = dpp_max<0x124>(v); v = dpp_max<0x122>(v); v = dpp_max<0x121>(v); return x32_max(x16_max(v)); }
DI f32x16 mfma16(u32x4 a, u32x4 b, f32x16 c) { return __builtin_amdgcn_mfma_f32_32x32x16_bf16(__builtin_bit_cast(bf16x8, a), __builtin_bit_cast(bf16x8, b), c, 0, 0, 0); }
DI f32x16 mfma8(u32x2 a, u32x2 b, f32x16 c) { return __builtin_amdgcn_mfma_f32_32x32x8bf16_1k(__builtin_bit_cast(s16x4, a), __builtin_bit_cast(s16x4, b), c, 0, 0, 0); }
DI f32x4 mfma_s(u32x4 a, u32x4 b, f32x4 c) { return __builtin_amdgcn_mfma_f32_16x16x32_bf16(__builtin_bit_cast(bf16x8, a), __builtin_bit_cast(bf16x8, b), c, 0, 0, 0); }
typedef short v4i16_t __attribute__((ext_vector_type(4)));
DI u32x2 lds_tr(u32 a) { return __builtin_bit_cast(u32x2, __builtin_amdgcn_ds_read_tr16_b64_v4i16((LAS v4i16_t*)a)); }
DI u32x4 lds128(u32 a) { return *(const LAS u32x4*)a; }
template <int IMM> DI void glds(u32 ldsbase, u32 voff, rsrc_t rs, u32 soff) {
    asm volatile("s_add_u32 m0, %0, %c4\n\ts_nop 0\n\tbuffer_load_dwordx4 %1, %2, %3 offen lds" ::"s"(ldsbase), "v"(voff), "s"(rs), "s"(soff), "i"(IMM) : "scc", "memory");
}
DI void glds_c(u32 ldsbase, u32 voff, rsrc_t rs, u32 soff) {
    asm volatile("s_mov_b32 m0, %0\n\ts_nop 0\n\tbuffer_load_dwordx4 %1, %2, %3 offen sc1 lds" ::"s"(ldsbase), "v"(voff), "s"(rs), "s"(soff) : "memory");
}
DI int fresh_lane() { int l = __builtin_amdgcn_mbcnt_hi(~0u, __builtin_amdgcn_mbcnt_lo(~0u, 0u)); asm volatile("" : "+v"(l)); return l; }
template <int N> DI void wait_vm() { asm volatile("s_waitcnt vmcnt(%c0)" ::"i"(N) : "memory"); }
template <int N> DI void wait_bar() { asm volatile("s_waitcnt vmcnt(%c0) lgkmcnt(0)\n\ts_barrier" ::"i"(N) : "memory"); }
#define SBAR() __builtin_amdgcn_sched_barrier(0)
DI u32 ld4_asm(rsrc_t rs, u32 voff, u32 soff) { u32 r; asm volatile("buffer_load_dword %0, %1, %2, %3 offen" : "=v"(r) : "v"(voff), "s"(rs), "s"(soff) : "memory"); return r; }
DI u32x2 ld8_asm(rsrc_t rs, u32 voff, u32 soff) { u32x2 r; asm volatile("buffer_load_dwordx2 %0, %1, %2, %3 offen" : "=v"(r) : "v"(voff), "s"(rs), "s"(soff) : "memory"); return r; }
DI u32x4 ld16c_asm(rsrc_t rs, u32 voff, u32 soff) { u32x4 r; asm volatile("buffer_load_dwordx4 %0, %1, %2, %3 offen sc1" : "=v"(r) : "v"(voff), "s"(rs), "s"(soff) : "memory"); return r; }
DI u32x4 ld16c(rsrc_t rs, u32 voff, u32 soff) { return __builtin_amdgcn_raw_buffer_load_b128(rs, voff, soff, AUX_SC1); }
DI u32x2 ld8c(rsrc_t rs, u32 voff, u32 soff) { return __builtin_amdgcn_raw_buffer_load_b64(rs, voff, soff, AUX_SC1); }
DI u32 ld4c(rsrc_t rs, u32 voff, u32 soff) { return __builtin_amdgcn_raw_buffer_load_b32(rs, voff, soff, AUX_SC1); }
DI void st16c(u32x4 v, rsrc_t rs, u32 voff, u32 soff) { __builtin_amdgcn_raw_buffer_store_b128(v, rs, voff + soff, 0, AUX_SC1); }
DI void st8c(u32x2 v, rsrc_t rs, u32 voff, u32 soff) { __builtin_amdgcn_raw_buffer_store_b64(v, rs, voff, soff, AUX_SC1); }
DI void st4c(u32 v, rsrc_t rs, u32 voff, u32 soff) { __builtin_amdgcn_raw_buffer_store_b32(v, rs, voff, soff, AUX_SC1); }
DI u32x2 split3(float v) {
    const float h = rbf(v), r1 = v - h, m = rbf(r1), r2 = r1 - m;
    return u32x2{pk2(h, m), pk2(r2, 0.f)};
}
template <int N> DI void wait_flags(rsrc_t ws, u32 off, u32 voff, u32 tag) {
#pragma unroll 1
    for (u32 spins = 0; spins < SPIN_CAP; ++spins) {
        bool ok = true;
        asm volatile("" ::: "memory");
#pragma unroll
        for (int i = 0; i < N; ++i) { const u32x4 f = ld16c(ws, voff + 1024 * i, off); ok &= (f[0] == tag) & (f[1] == ~tag) & (f[2] == tag) & (f[3] == ~tag); }
        if (__all(ok)) break;
        __builtin_amdgcn_s_sleep(2);
    }
    asm volatile("" ::: "memory");
}

constexpr int fills_upto(int s) { return s <= 0 ? 0 : (s <= 10 ? 2 * s : s + 10); }
constexpr int issued_after(int s) { return 8 + fills_upto(s) > 30 ? 30 : 8 + fills_upto(s); }

#define STAMP(i)
#define STAMP_FLUSH()

template <int MASK>
__global__ __launch_bounds__(512, 2) void fused_kernel(const u16* __restrict__ x, const u16* __restrict__ wq, const u16* __restrict__ wk, const u16* __restrict__ wv,
                                                       const u16* __restrict__ wo, const u16* __restrict__ wg, const u16* __restrict__ wu, const u16* __restrict__ wd,
                                                       u16* __restrict__ out, char* __restrict__ wsp) {
    constexpr bool FUSED = MASK == 15;
    __shared__ __attribute__((aligned(1024))) char smem[L_TOTAL];
    const u32 lds0 = (u32)(uintptr_t)(LAS char*)smem;
    const int tid = threadIdx.x, lane = tid & 63, w = __builtin_amdgcn_readfirstlane(tid >> 6);
    const int b = blockIdx.x;
    const int r32 = lane & 31, hh = lane >> 5;
    const rsrc_t ws = mkrs(wsp, WS_END);
    const rsrc_t rsx = mkrs(x, S_ * D_ * 2);
    u32 tag = 1;
    if constexpr (MASK & 1) STAMP(0);
    if constexpr (MASK == 2) STAMP(10);
    if constexpr (MASK == 4) { if (w < 4) STAMP(11); }
    if constexpr (MASK == 8) { if (w >= 4) STAMP(11); }

    if constexpr ((MASK & 3) != 0) {
        const int xcd = b & 7, j = b >> 3;
        const int rb = 8 * (xcd >> 2) + (j >> 2), cg = 4 * (xcd & 3) + (j & 3);
        const int tg = w & 3, wc = w >> 2;
        const bool kvwg = cg >= 8;
        const int kvtype = cg & 1, kvtg = (cg >> 1) & 3;
        const bool kvwave = kvwg && w == kvtg;
        const bool u1q = cg < 5;
        const rsrc_t rg = mkrs(wg, D_ * F_ * 2), ru = mkrs(wu, D_ * F_ * 2);
        const rsrc_t r10 = u1q ? mkrs(wq, D_ * D_ * 2) : rg, r11 = u1q ? mkrs(wq, D_ * D_ * 2) : ru;
        const u32 st1 = u1q ? D_ * 2 : F_ * 2;
        const u32 c10 = u1q ? cg * 256 : (cg - 5) * 128, c11 = u1q ? c10 + 128 : c10;
        const u32 c2 = (cg + 11) * 128;

        u32 ep = 0, xs[5]; u32x2 cw[5], ck[5];
        const int srow = rb * 128 + cg * 8 + w;
        const int usel = rb & 1, chalf = (rb >> 1) & 1, kq = rb >> 2;
        const bool kvcs = cg == 5 && rb < 8;
        if constexpr (MASK & 1) {
            if constexpr (FUSED) asm volatile("buffer_load_dword %0, off, %1, 0 sc1" : "=v"(ep) : "s"(ws) : "memory");
#pragma unroll
            for (int i = 0; i < 5; ++i) xs[i] = ld4_asm(rsx, lane * 4 + i * 256, srow * 1280);
            const rsrc_t rc = usel ? (chalf ? ru : rg) : (chalf ? r11 : r10);
            const u32 stc = usel ? F_ * 2 : st1, cc = usel ? c2 : (chalf ? c11 : c10);
#pragma unroll
            for (int i = 0; i < 5; ++i) cw[i] = ld8_asm(rc, (lane >> 4) * stc + (lane & 15) * 8, (kq * 160 + w * 20 + 4 * i) * stc + cc);
            const rsrc_t rk = mkrs((rb & 1) ? wv : wk, kvcs ? D_ * 40 : 0);
#pragma unroll
            for (int i = 0; i < 5; ++i) ck[i] = ld8_asm(rk, (lane >> 4) * 40 + (lane & 15) * 8, ((rb >> 1) * 160 + w * 20 + 4 * i) * 40);
        }
        const u32 vx0 = (16 * w + (lane >> 3)) * 1280 + (((lane & 7) ^ (lane >> 4)) * 16);
        const u32 vx1 = (16 * w + 8 + (lane >> 3)) * 1280 + (((lane & 7) ^ (4 + (lane >> 4))) * 16);
        const u32 wsw0 = ((((lane >> 1) & 3) ^ ((lane >> 4) & 1)) * 32) + (lane & 1) * 16, wsw1 = wsw0 ^ 64;
        const u32 wrow = 8 * w + (lane >> 3);
        const u32 vw10 = wrow * st1 + wsw0, vw11 = wrow * st1 + wsw1, vw20 = wrow * (F_ * 2) + wsw0, vw21 = wrow * (F_ * 2) + wsw1;
        const u32 ldw = lds0 + L_RING + w * 2048;
        const u32 sx = rb * 128 * 1280;
        auto fill = [&](auto fi) {
            constexpr int f = decltype(fi)::value, slot = (f & 7) * 16384;
            if constexpr (f < 20 && (f & 1) == 0) { glds<slot>(ldw, vx0, rsx, sx + (f >> 1) * 128); glds<slot + 1024>(ldw, vx1, rsx, sx + (f >> 1) * 128); }
            else if constexpr (f < 20) { glds<slot>(ldw, vw10, r10, c10 + (f >> 1) * 64 * st1); glds<slot + 1024>(ldw, vw11, r11, c11 + (f >> 1) * 64 * st1); }
            else { glds<slot>(ldw, vw20, rg, c2 + (f - 20) * 64 * F_ * 2); glds<slot + 1024>(ldw, vw21, ru, c2 + (f - 20) * 64 * F_ * 2); }
        };
        if constexpr (MASK & 2) {
            if (kvwg) {
                const rsrc_t rkv = mkrs(kvtype ? wv : wk, D_ * 40);
                const u32 lk = lds0 + L_WKV + w * 1024;
                glds<0>(lk, lane * 16, rkv, w * 1024); glds<8192>(lk, lane * 16, rkv, w * 1024 + 8192); glds<16384>(lk, lane * 16, rkv, w * 1024 + 16384);
                if (w == 0) glds<24576>(lk, lane * 16, rkv, 24576);
            }
            sfor<8>(fill);
        }
        if constexpr (MASK & 1) {
            asm volatile("s_waitcnt vmcnt(%c16)" : "+v"(ep), "+v"(xs[0]), "+v"(xs[1]), "+v"(xs[2]), "+v"(xs[3]), "+v"(xs[4]), "+v"(cw[0]), "+v"(cw[1]), "+v"(cw[2]), "+v"(cw[3]), "+v"(cw[4]),
                         "+v"(ck[0]), "+v"(ck[1]), "+v"(ck[2]), "+v"(ck[3]), "+v"(ck[4]) : "i"((MASK & 2) ? 16 : 0) : "memory");
            if constexpr (FUSED) tag = __builtin_amdgcn_readfirstlane(ep) + 1;
            float s = 0.f;
#pragma unroll
            for (int i = 0; i < 5; ++i) s += bflo(xs[i]) + bfhi(xs[i]);
            const float mean = wave_sum(s) * (1.f / D_);
            float q = 0.f;
#pragma unroll
            for (int i = 0; i < 5; ++i) { const float a = bflo(xs[i]) - mean, c = bfhi(xs[i]) - mean; q += a * a + c * c; }
            const float var = wave_sum(q) * (1.f / D_);
            const float sd = rbf(__builtin_sqrtf(rbf(rbf(var) + 1e-5f)));
            if (lane == 0) st16c(u32x4{__float_as_uint(rbf(mean)), tag, __float_as_uint(1.f / sd), ~tag}, ws, 0, WS_STAT + srow * 16);
            auto colsum = [&](const u32x2 (&c)[5], u32 scr) {
                f32x4 a = {0.f, 0.f, 0.f, 0.f};
#pragma unroll
                for (int i = 0; i < 5; ++i) { a[0] += bflo(c[i][0]); a[1] += bfhi(c[i][0]); a[2] += bflo(c[i][1]); a[3] += bfhi(c[i][1]); }
#pragma unroll
                for (int e = 0; e < 4; ++e) a[e] = x32_add(x16_add(a[e]));
                if (lane < 16) *(LAS f32x4*)(scr + w * 256 + lane * 16) = a;
            };
            colsum(cw, lds0 + L_SCR);
            if (kvcs) colsum(ck, lds0 + L_SCR + 2048);
        }
        wait_bar<(MASK & 2) ? 12 : 0>();
        if constexpr (MASK & 2) STAMP(1);
        if constexpr (MASK & 1) {
            if (w == 7 || (w == 6 && kvcs)) {
                const u32 scr = lds0 + L_SCR + (w == 6 ? 2048 : 0);
                float a = 0.f;
#pragma unroll
                for (int i = 0; i < 8; ++i) a += *(const LAS float*)(scr + i * 256 + lane * 4);
                auto r = __builtin_amdgcn_permlane16_swap(__float_as_uint(a), __float_as_uint(a), false, false);
                const u32 dst = w == 6 ? (64 * 4 + (rb >> 1)) * 512 + (rb & 1) * 256 : (((cg + 16 * usel) * 2 + chalf) * 4 + kq) * 512;
                if ((lane & 16) == 0 && (w == 7 || lane < 16)) st16c(u32x4{r[0], tag, r[1], ~tag}, ws, (hh * 16 + (lane & 15)) * 16, WS_CS + dst);
            }
        }
        u32 wt[2][8];
        auto wt_load = [&]() {
            const rsrc_t rd = mkrs(wd, F_ * D_ * 2), ro = mkrs(wo, D_ * D_ * 2);
#pragma unroll
            for (int it = 0; it < 2; ++it) {
                const int item = b * 8 + w + 2048 * it, X = item / 10, cb = item - X * 10;
                if (item < 2960) {
                    const int Y = X - 216, hg = Y / 10, jp = (Y - hg * 10) / 5, ci = Y - hg * 10 - jp * 5, hb = 8 * (hg >> 1) + (hg & 1) + 4 * jp;
                    const bool isd = X < 216;
                    const rsrc_t rr = isd ? rd : ro;
                    const int rowa = isd ? 8 * X : (ci < 4 ? (hb + 2 * (ci >> 1)) * 20 + 4 * (ci & 1) : hb * 20 + 16);
                    const int rowb = isd ? rowa + 4 : (ci < 4 ? rowa + 8 : rowa + 40);
                    const u32 vo = cb * 128 + lane * 2;
                    asm volatile("buffer_load_ushort %0, %8, %9, %10 offen\n\tbuffer_load_ushort %1, %8, %9, %10 offen offset:1280\n\t"
                                 "buffer_load_ushort %2, %8, %9, %10 offen offset:2560\n\tbuffer_load_ushort %3, %8, %9, %10 offen offset:3840\n\t"
                                 "buffer_load_ushort %4, %8, %9, %11 offen\n\tbuffer_load_ushort %5, %8, %9, %11 offen offset:1280\n\t"
                                 "buffer_load_ushort %6, %8, %9, %11 offen offset:2560\n\tbuffer_load_ushort %7, %8, %9, %11 offen offset:3840"
                                 : "=&v"(wt[it][0]), "=&v"(wt[it][1]), "=&v"(wt[it][2]), "=&v"(wt[it][3]), "=&v"(wt[it][4]), "=&v"(wt[it][5]), "=&v"(wt[it][6]), "=&v"(wt[it][7])
                                 : "v"(vo), "s"(rr), "s"(rowa * 1280), "s"(rowb * 1280) : "memory");
                }
            }
        };
        auto wt_store = [&]() {
#pragma unroll
            for (int it = 0; it < 2; ++it) {
                const int item = b * 8 + w + 2048 * it, X = item / 10, cb = item - X * 10;
                if (item < 2960) {
                    asm volatile("" : "+v"(wt[it][0]), "+v"(wt[it][1]), "+v"(wt[it][2]), "+v"(wt[it][3]), "+v"(wt[it][4]), "+v"(wt[it][5]), "+v"(wt[it][6]), "+v"(wt[it][7]));
                    st16c(u32x4{wt[it][0] | (wt[it][1] << 16), wt[it][2] | (wt[it][3] << 16), wt[it][4] | (wt[it][5] << 16), wt[it][6] | (wt[it][7] << 16)}, ws, (lane >> 4) * (296 * 256) + (lane & 15) * 16, WS_WT + cb * 4 * 296 * 256 + X * 256);
                }
            }
        };
        if constexpr (MASK == 1) { wt_load(); wait_vm<0>(); wt_store(); }

        if constexpr (MASK & 2) {
            const int tq = (lane >> 2) & 3, tp = lane & 3, tgrp = (lane >> 4) & 1;
            const u32 wa_ = lds0 + L_RING + hh * 2048 + (tp >> 1) * 1024 + tq * 128 + 16 * (tp & 1) + 8 * tgrp;
            const u32 wx_ = (tp & 2) | (tq >> 1);
            const u32 wa0 = wa_ + ((2 * wc) ^ wx_) * 32, wa1 = wa_ + ((2 * wc + 1) ^ wx_) * 32;
            const u32 xa_ = lds0 + L_RING + (tg * 32 + r32) * 128, xsw = (r32 >> 1) & 7;
            const int kg = 2 * tgrp + (tp >> 1), kcol = kg + 2 * (tp & 1) > 4 ? 4 : kg + 2 * (tp & 1);
            const u32 ka = lds0 + L_WKV + (8 * hh + tq) * 40 + kcol * 8;
            u32x4 xf[40], wf[2][2];
            f32x16 acc[2], kacc;
            kacc = f32x16{};
            float rstd; u32x2 bm;
            auto rd_frags = [&](auto ti) {
                constexpr int t = decltype(ti)::value, s = t >> 2, ks = t & 3;
                constexpr int wslot = ((s < 10 ? 2 * s + 1 : s + 10) & 7) * 16384 + ks * 4096;
                if constexpr (s < 10) xf[t] = lds128(xa_ + ((2 * s) & 7) * 16384 + (((2 * ks + hh) ^ xsw) * 16));
                const u32x2 a0 = lds_tr(wa0 + wslot), a1 = lds_tr(wa0 + wslot + 512), b0 = lds_tr(wa1 + wslot), b1 = lds_tr(wa1 + wslot + 512);
                wf[t & 1][0] = u32x4{a0[0], a0[1], a1[0], a1[1]};
                wf[t & 1][1] = u32x4{b0[0], b0[1], b1[0], b1[1]};
            };
            auto gran_ok = [&](const u32x4& g) { return (g[1] == tag) & (g[3] == ~tag); };
            auto epilogue = [&](auto ui) {
                constexpr int u = decltype(ui)::value;
                const int ln = fresh_lane(), r32 = ln & 31, hh = ln >> 5, lane = ln;
                const u32 vgc = (((r32 >> 3) & 1) * 4 + 2 * hh) * 512 + (wc * 16 + 8 * ((r32 >> 2) & 1) + 4 * (r32 >> 4) + (r32 & 3)) * 16;
                constexpr u32 lgc = u == 0 ? L_SCR + 2048 : L_WKV + 4096;
                u32x4 gst, gc[2];
                if constexpr (u == 0) gst = lds128(lds0 + L_SCR + (tg * 32 + r32) * 16);
                gc[0] = lds128(lds0 + lgc + vgc); gc[1] = lds128(lds0 + lgc + vgc + 512);
                if constexpr (FUSED) {
#pragma unroll 1
                    for (u32 spins = 0; spins < SPIN_CAP; ++spins) {
                        bool ok = gran_ok(gc[0]) & gran_ok(gc[1]);
                        if constexpr (u == 0) ok &= gran_ok(gst);
                        if (__all(ok)) break;
                        asm volatile("" ::: "memory");
                        if constexpr (u == 0) gst = ld16c(ws, r32 * 16, WS_STAT + (rb * 128 + tg * 32) * 16);
                        gc[0] = ld16c(ws, vgc, WS_CS + (cg + 16 * u) * 4096); gc[1] = ld16c(ws, vgc + 512, WS_CS + (cg + 16 * u) * 4096);
                    }
                }
                if constexpr (u == 0) {
                    const u32 nm = (gst[0] >> 16) ^ 0x8000u;
                    bm = u32x2{nm | (nm << 16), nm};
                    rstd = __uint_as_float(gst[2]);
                }
                acc[0] = mfma8(split3(__uint_as_float(gc[0][0]) + __uint_as_float(gc[1][0])), bm, acc[0]);
                acc[1] = mfma8(split3(__uint_as_float(gc[0][2]) + __uint_as_float(gc[1][2])), bm, acc[1]);
                if (u == 0 && u1q) {
                    const float sc = rstd * (0.22360679774997896f * 1.4426950408889634f);
#pragma unroll
                    for (int t = 0; t < 2; ++t)
#pragma unroll
                        for (int hf = 0; hf < 2; ++hf) {
                            const f32x16& a = acc[t];
                            const u32x4 v = {pk2(a[4 * hf] * sc, a[4 * hf + 1] * sc), pk2(a[4 * hf + 2] * sc, a[4 * hf + 3] * sc),
                                             pk2(a[8 + 4 * hf] * sc, a[9 + 4 * hf] * sc), pk2(a[10 + 4 * hf] * sc, a[11 + 4 * hf] * sc)};
                            st16c(v, ws, lane * 16, WS_Q + (rb * 4 + tg) * 40960 + (cg * 16 + hf * 8 + 2 * (2 * wc + t)) * 512);
                        }
                } else {
                    const float r2 = rstd * rstd;
                    const int fu = u == 0 ? cg - 5 : cg + 11;
#pragma unroll
                    for (int t = 0; t < 2; ++t) {
                        const f32x16& a = acc[t];
                        float hv[8];
#pragma unroll
                        for (int e = 0; e < 4; ++e) { hv[e] = __builtin_fmaxf(a[e], 0.f) * r2 * a[4 + e]; hv[4 + e] = __builtin_fmaxf(a[8 + e], 0.f) * r2 * a[12 + e]; }
                        const u32x4 v = {pk2(hv[0], hv[1]), pk2(hv[2], hv[3]), pk2(hv[4], hv[5]), pk2(hv[6], hv[7])};
                        st16c(v, ws, (r32 >> 4) * (296 * 256) + hh * 256 + (r32 & 15) * 16, WS_ACT + (rb * 8 + tg * 2) * (296 * 256) + (fu * 8 + 2 * (2 * wc + t)) * 256);
                    }
                }
                if (u == 0 && kvwave) {
                    SBAR();
                    const int kvc_ = (r32 >> 3) + 2 * ((r32 >> 2) & 1), kvc = 4 * (kvc_ > 4 ? 4 : kvc_) + (r32 & 3);
                    u32x4 gk[2];
                    gk[0] = lds128(lds0 + L_SCR + 6144 + hh * 512 + (kvc & 15) * 16); gk[1] = lds128(lds0 + L_SCR + 6144 + 256 + hh * 512 + (kvc & 15) * 16);
                    if constexpr (FUSED) {
#pragma unroll 1
                        for (u32 spins = 0; spins < SPIN_CAP; ++spins) {
                            if (__all(gran_ok(gk[0]) & gran_ok(gk[1]))) break;
                            asm volatile("" ::: "memory");
                            gk[0] = ld16c(ws, hh * 1024 + (kvc & 15) * 16, WS_CS + 64 * 2048 + kvtype * 256); gk[1] = ld16c(ws, hh * 1024 + 512 + (kvc & 15) * 16, WS_CS + 64 * 2048 + kvtype * 256);
                        }
                    }
                    const float c = (kvc & 16) ? __uint_as_float(gk[0][2]) + __uint_as_float(gk[1][2]) : __uint_as_float(gk[0][0]) + __uint_as_float(gk[1][0]);
                    kacc = mfma8(split3(c), bm, kacc);
                    u32 p[6];
#pragma unroll
                    for (int e = 0; e < 6; ++e) p[e] = pk2(kacc[2 * e] * rstd, kacc[2 * e + 1] * rstd);
                    const u32 kt = WS_KV + (rb * 4 + tg) * 5120;
                    if (kvtype == 0) {
                        st16c(u32x4{p[0], p[1], p[2], p[3]}, ws, lane * 16, kt);
                        st16c(u32x4{p[2], p[3], p[4], p[5]}, ws, lane * 16, kt + 1536);
                        st8c(hh ? u32x2{p[4], p[5]} : u32x2{p[0], p[1]}, ws, r32 * 8 + (hh ? 1024 : 2560), kt);
                        st8c(u32x2{0x3f803f80u, 0x00003f80u}, ws, r32 * 8 + (hh ? 1280 : 2816), kt);
                        float n2 = 0.f;
#pragma unroll
                        for (int e = 0; e < 6; ++e) { const float lo = bflo(p[e]), hi = bfhi(p[e]); const float t2 = lo * lo + hi * hi; n2 += (e < 4 || hh) ? t2 : 0.f; }
                        n2 = wave_max(x32_add(n2));
                        if (lane == 0) st4c(__float_as_uint(n2), ws, 0, WS_KMAX + (rb * 4 + tg) * 4);
                    } else {
                        const u32 vs = lds0 + L_WKV;
#pragma unroll
                        for (int e = 0; e < 6; ++e) {
                            *(LAS u16*)(vs + (8 * hh + 2 * e) * 80 + r32 * 2) = (u16)p[e];
                            *(LAS u16*)(vs + (8 * hh + 2 * e + 1) * 80 + r32 * 2) = (u16)(p[e] >> 16);
                        }
                        const int vr = r32 < 20 ? r32 : 0;
#pragma unroll
                        for (int s2 = 0; s2 < 2; ++s2) {
                            u32x4 v = lds128(vs + vr * 80 + s2 * 32 + hh * 16);
                            const u32 fillv = r32 == 20 ? 0x3f803f80u : 0u;
                            if (r32 >= 20) v = u32x4{fillv, fillv, fillv, fillv};
                            st16c(v, ws, lane * 16, kt + 3072 + s2 * 1024);
                        }
                    }
                }
            };
            rd_frags(IC<0>{});
            sfor<80>([&](auto ti) {
                constexpr int t = decltype(ti)::value, s = t >> 2, ks = t & 3;
                if constexpr (ks == 3 && s < 14) {
                    constexpr int allow = 2 * (issued_after(s) - fills_upto(s + 2));
                    constexpr int allow2 = (s == 12 && FUSED) ? 2 * (issued_after(12) - issued_after(10))
                                           : s == 9 ? 2 * (issued_after(9) - issued_after(7))
                                           : s == 13 ? 0 : allow;
                    wait_bar<(allow2 < allow ? allow2 : allow)>();
                    if constexpr (s == 12 && FUSED) { if (tid == 0) st8c(u32x2{tag, ~tag}, ws, 0, WS_FLAGA + b * 8); }
                    if constexpr (s == 7) {
                        const int ln = fresh_lane(), wp = w < 6 ? w : 6;
                        glds_c(lds0 + L_SCR + wp * 1024, w < 6 ? ln * 16 : (ln >> 4) * 512 + (ln & 15) * 16, ws,
                               w < 2 ? WS_STAT + rb * 2048 + w * 1024 : w < 6 ? WS_CS + cg * 4096 + (w - 2) * 1024 : WS_CS + 64 * 2048 + kvtype * 256);
                    }
                    if constexpr (s == 10) glds_c(lds0 + L_WKV + 4096 + (w & 3) * 1024, fresh_lane() * 16, ws, WS_CS + (cg + 16) * 4096 + (w & 3) * 1024);
                    sfor<issued_after(s + 1) - issued_after(s)>([&](auto k) { fill(IC<issued_after(s) + decltype(k)::value>{}); });
                    if constexpr (s == 13 && FUSED) wt_load();
                }
                if constexpr (t < 79) rd_frags(IC<t + 1>{});
                SBAR();
                if constexpr (t == 0 || t == 40) { acc[0] = mfma16(wf[t & 1][0], xf[t % 40], f32x16{}); acc[1] = mfma16(wf[t & 1][1], xf[t % 40], f32x16{}); }
                else { acc[0] = mfma16(wf[t & 1][0], xf[t % 40], acc[0]); acc[1] = mfma16(wf[t & 1][1], xf[t % 40], acc[1]); }
                SBAR();
                if constexpr (ks == 3 && s < 10) {
                    if (kvwave) {
#pragma unroll
                        for (int k2 = 0; k2 < 4; ++k2) {
                            const u32x2 a0 = lds_tr(ka + (s * 64 + k2 * 16) * 40), a1 = lds_tr(ka + (s * 64 + k2 * 16 + 4) * 40);
                            kacc = mfma16(u32x4{a0[0], a0[1], a1[0], a1[1]}, xf[4 * s + k2], kacc);
                        }
                    }
                    SBAR();
                }
                if constexpr (t == 39) {
                    STAMP(2);
                    epilogue(IC<0>{});
                    SBAR();
                }
            });
            STAMP(3);
            if constexpr (FUSED) { wait_vm<0>(); wt_store(); }
            epilogue(IC<1>{});
            if constexpr (FUSED) {
                wait_bar<0>();
                if (tid == 0) st8c(u32x2{tag, ~tag}, ws, 0, WS_FLAGB + b * 8);
            }
            STAMP(4);
        }
    }

    if constexpr ((MASK & 12) != 0) {
        const int tb = (b & 7) * 4 + ((b >> 3) & 3), hg = b >> 5;
        f32x4 acc2[5][4];
        if (w < 4) {
            if constexpr (MASK & 4) {
                if constexpr (FUSED) wait_flags<2>(ws, WS_FLAGA, lane * 16, tag);
                STAMP(5);
                const int par = hg & 1;
                u32x32 q0; u32x16 q1;
                const float k2 = wave_max(__uint_as_float(ld4c(ws, lane * 4, WS_KMAX)));
                float mx[8];
#pragma unroll
                for (int i = 0; i < 8; ++i) {
                    const int rt = (i + 2 * w) & 7, tt = rt >> 2, P = 4 * (hg >> 1) + (rt & 3);
                    const u32 so = WS_Q + (tb * 2 + tt) * 40960 + 5 * P * 512;
                    const u32x4 a = ld16c(ws, lane * 16, so + par * 1536);
                    u32x2 c = ld8c(ws, r32 * 16 + par * 8, so + 1024);
                    if (hh) c = u32x2{0u, 0u};
                    float n2 = 0.f;
#pragma unroll
                    for (int e = 0; e < 4; ++e) { n2 = __builtin_amdgcn_fdot2_f32_bf16(__builtin_bit_cast(bf16x2, a[e]), __builtin_bit_cast(bf16x2, a[e]), n2, false); q0[4 * i + e] = a[e]; }
#pragma unroll
                    for (int e = 0; e < 2; ++e) { n2 = __builtin_amdgcn_fdot2_f32_bf16(__builtin_bit_cast(bf16x2, c[e]), __builtin_bit_cast(bf16x2, c[e]), n2, false); q1[2 * i + e] = c[e]; }
                    mx[i] = __builtin_sqrtf(x32_add(n2) * k2) * 1.001f;
                }
                const u32 vk0 = lane * 16 + par * 1536, vk1 = lane * 8 + 1024 + par * 1536, vv = lane * 16 + 3072;
                bool safe = true;
#pragma unroll
                for (int i = 0; i < 8; ++i) safe &= mx[i] <= 50.f;
                if (!__all(safe)) {
#pragma unroll
                    for (int i = 0; i < 8; ++i) mx[i] = -INFINITY;
                    for (int kt = 0; kt < 64; ++kt) {
                        const u32x4 k0 = ld16c(ws, vk0, WS_KV + kt * 5120); const u32x2 k1 = ld8c(ws, vk1, WS_KV + kt * 5120);
#pragma unroll
                        for (int i = 0; i < 8; ++i) {
                            f32x16 sc = mfma16(k0, u32x4{q0[4 * i], q0[4 * i + 1], q0[4 * i + 2], q0[4 * i + 3]}, f32x16{});
                            sc = mfma8(k1, u32x2{q1[2 * i], q1[2 * i + 1]}, sc);
#pragma unroll
                            for (int e = 0; e < 16; ++e) mx[i] = __builtin_fmaxf(mx[i], sc[e]);
                        }
                    }
#pragma unroll
                    for (int i = 0; i < 8; ++i) mx[i] = x32_max(mx[i]);
                }
#pragma unroll
                for (int i = 0; i < 8; ++i) { const u32x2 m3 = split3(-mx[i]); if (hh) { q1[2 * i] = m3[0]; q1[2 * i + 1] = m3[1]; } }
                f32x32 o0, o1, o2, o3;
                u32 so = WS_KV + w * 16 * 5120, cnt = 8;
                STAMP(6);
                asm volatile(
      "buffer_load_dwordx4 v[208:211], %[vk0], %[rs], %[so] offen sc1\n"
      "buffer_load_dwordx2 v[220:221], %[vk1], %[rs], %[so] offen sc1\n"
      "buffer_load_dwordx4 v[212:215], %[vv], %[rs], %[so] offen sc1\n"
      "buffer_load_dwordx4 v[216:219], %[vv], %[rs], %[so] offen offset:1024 sc1\n"
      "s_add_u32 %[so], %[so], 5120\n"
      "v_mov_b32 v236, 0xdddd\n"
      "v_mov_b32 v0, 0\n"
      "v_mov_b32 v1, 0\n"
      "v_mov_b32 v2, 0\n"
      "v_mov_b32 v3, 0\n"
      "v_mov_b32 v4, 0\n"
      "v_mov_b32 v5, 0\n"
      "v_mov_b32 v6, 0\n"
      "v_mov_b32 v7, 0\n"
      "v_mov_b32 v8, 0\n"
      "v_mov_b32 v9, 0\n"
      "v_mov_b32 v10, 0\n"
      "v_mov_b32 v11, 0\n"
      "v_mov_b32 v12, 0\n"
      "v_mov_b32 v13, 0\n"
      "v_mov_b32 v14, 0\n"
      "v_mov_b32 v15, 0\n"
      "v_mov_b32 v16, 0\n"
      "v_mov_b32 v17, 0\n"
      "v_mov_b32 v18, 0\n"
      "v_mov_b32 v19, 0\n"
      "v_mov_b32 v20, 0\n"
      "v_mov_b32 v21, 0\n"
      "v_mov_b32 v22, 0\n"
      "v_mov_b32 v23, 0\n"
      "v_mov_b32 v24, 0\n"
      "v_mov_b32 v25, 0\n"
      "v_mov_b32 v26, 0\n"
      "v_mov_b32 v27, 0\n"
      "v_mov_b32 v28, 0\n"
      "v_mov_b32 v29, 0\n"
      "v_mov_b32 v30, 0\n"
      "v_mov_b32 v31, 0\n"
      "v_mov_b32 v32, 0\n"
      "v_mov_b32 v33, 0\n"
      "v_mov_b32 v34, 0\n"
      "v_mov_b32 v35, 0\n"
      "v_mov_b32 v36, 0\n"
      "v_mov_b32 v37, 0\n"
      "v_mov_b32 v38, 0\n"
      "v_mov_b32 v39, 0\n"
      "v_mov_b32 v40, 0\n"
      "v_mov_b32 v41, 0\n"
      "v_mov_b32 v42, 0\n"
      "v_mov_b32 v43, 0\n"
      "v_mov_b32 v44, 0\n"
      "v_mov_b32 v45, 0\n"
      "v_mov_b32 v46, 0\n"
      "v_mov_b32 v47, 0\n"
      "v_mov_b32 v48, 0\n"
      "v_mov_b32 v49, 0\n"
      "v_mov_b32 v50, 0\n"
      "v_mov_b32 v51, 0\n"
      "v_mov_b32 v52, 0\n"
      "v_mov_b32 v53, 0\n"
      "v_mov_b32 v54, 0\n"
      "v_mov_b32 v55, 0\n"
      "v_mov_b32 v56, 0\n"
      "v_mov_b32 v57, 0\n"
      "v_mov_b32 v58, 0\n"
      "v_mov_b32 v59, 0\n"
      "v_mov_b32 v60, 0\n"
      "v_mov_b32 v61, 0\n"
      "v_mov_b32 v62, 0\n"
      "v_mov_b32 v63, 0\n"
      "v_mov_b32 v64, 0\n"
      "v_mov_b32 v65, 0\n"
      "v_mov_b32 v66, 0\n"
      "v_mov_b32 v67, 0\n"
      "v_mov_b32 v68, 0\n"
      "v_mov_b32 v69, 0\n"
      "v_mov_b32 v70, 0\n"
      "v_mov_b32 v71, 0\n"
      "v_mov_b32 v72, 0\n"
      "v_mov_b32 v73, 0\n"
      "v_mov_b32 v74, 0\n"
      "v_mov_b32 v75, 0\n"
      "v_mov_b32 v76, 0\n"
      "v_mov_b32 v77, 0\n"
      "v_mov_b32 v78, 0\n"
      "v_mov_b32 v79, 0\n"
      "v_mov_b32 v80, 0\n"
      "v_mov_b32 v81, 0\n"
      "v_mov_b32 v82, 0\n"
      "v_mov_b32 v83, 0\n"
      "v_mov_b32 v84, 0\n"
      "v_mov_b32 v85, 0\n"
      "v_mov_b32 v86, 0\n"
      "v_mov_b32 v87, 0\n"
      "v_mov_b32 v88, 0\n"
      "v_mov_b32 v89, 0\n"
      "v_mov_b32 v90, 0\n"
      "v_mov_b32 v91, 0\n"
      "v_mov_b32 v92, 0\n"
      "v_mov_b32 v93, 0\n"
      "v_mov_b32 v94, 0\n"
      "v_mov_b32 v95, 0\n"
      "v_mov_b32 v96, 0\n"
      "v_mov_b32 v97, 0\n"
      "v_mov_b32 v98, 0\n"
      "v_mov_b32 v99, 0\n"
      "v_mov_b32 v100, 0\n"
      "v_mov_b32 v101, 0\n"
      "v_mov_b32 v102, 0\n"
      "v_mov_b32 v103, 0\n"
      "v_mov_b32 v104, 0\n"
      "v_mov_b32 v105, 0\n"
      "v_mov_b32 v106, 0\n"
      "v_mov_b32 v107, 0\n"
      "v_mov_b32 v108, 0\n"
      "v_mov_b32 v109, 0\n"
      "v_mov_b32 v110, 0\n"
      "v_mov_b32 v111, 0\n"
      "v_mov_b32 v112, 0\n"
      "v_mov_b32 v113, 0\n"
      "v_mov_b32 v114, 0\n"
      "v_mov_b32 v115, 0\n"
      "v_mov_b32 v116, 0\n"
      "v_mov_b32 v117, 0\n"
      "v_mov_b32 v118, 0\n"
      "v_mov_b32 v119, 0\n"
      "v_mov_b32 v120, 0\n"
      "v_mov_b32 v121, 0\n"
      "v_mov_b32 v122, 0\n"
      "v_mov_b32 v123, 0\n"
      "v_mov_b32 v124, 0\n"
      "v_mov_b32 v125, 0\n"
      "v_mov_b32 v126, 0\n"
      "v_mov_b32 v127, 0\n"
      "v_mov_b32 v200, 0\n"
      "v_mov_b32 v201, 0\n"
      "v_mov_b32 v202, 0\n"
      "v_mov_b32 v203, 0\n"
      "v_mov_b32 v204, 0\n"
      "v_mov_b32 v205, 0\n"
      "v_mov_b32 v206, 0\n"
      "v_mov_b32 v207, 0\n"
      "v_mov_b32 v230, 0\n"
      "v_mov_b32 v231, 0\n"
      "v_mov_b32 v232, 0\n"
      "v_mov_b32 v233, 0\n"
      "s_waitcnt vmcnt(0)\n"
      "v_mfma_f32_32x32x16_bf16 v[176:191], v[208:211], v[128:131], 0\n"
      "v_mfma_f32_32x32x8_bf16 v[176:191], v[220:221], v[160:161], v[176:191]\n"
      "s_nop 15\n"
      "1:\n"
      "v_exp_f32 v176, v176\n"
      "v_exp_f32 v177, v177\n"
      "v_smfmac_f32_32x32x32_bf16 v[112:127], v[230:233], v[200:207], v236\n"
      "v_exp_f32 v178, v178\n"
      "v_exp_f32 v179, v179\n"
      "v_exp_f32 v180, v180\n"
      "v_mfma_f32_32x32x16_bf16 v[192:207], v[208:211], v[132:135], 0\n"
      "v_exp_f32 v181, v181\n"
      "v_exp_f32 v182, v182\n"
      "v_exp_f32 v183, v183\n"
      "v_mfma_f32_32x32x8_bf16 v[192:207], v[220:221], v[162:163], v[192:207]\n"
      "v_exp_f32 v184, v184\n"
      "v_exp_f32 v185, v185\n"
      "v_exp_f32 v186, v186\n"
      "v_exp_f32 v187, v187\n"
      "v_smfmac_f32_32x32x32_bf16 v[0:15], v[212:215], v[176:183], v236\n"
      "v_exp_f32 v188, v188\n"
      "v_exp_f32 v189, v189\n"
      "v_exp_f32 v190, v190\n"
      "v_exp_f32 v191, v191\n"
      "v_exp_f32 v192, v192\n"
      "v_exp_f32 v193, v193\n"
      "v_smfmac_f32_32x32x32_bf16 v[0:15], v[216:219], v[184:191], v236\n"
      "buffer_load_dwordx4 v[222:225], %[vk0], %[rs], %[so] offen sc1\n"
      "v_exp_f32 v194, v194\n"
      "v_exp_f32 v195, v195\n"
      "v_exp_f32 v196, v196\n"
      "v_mfma_f32_32x32x16_bf16 v[176:191], v[208:211], v[136:139], 0\n"
      "buffer_load_dwordx2 v[234:235], %[vk1], %[rs], %[so] offen sc1\n"
      "v_exp_f32 v197, v197\n"
      "v_exp_f32 v198, v198\n"
      "v_exp_f32 v199, v199\n"
      "v_mfma_f32_32x32x8_bf16 v[176:191], v[220:221], v[164:165], v[176:191]\n"
      "buffer_load_dwordx4 v[226:229], %[vv], %[rs], %[so] offen sc1\n"
      "v_exp_f32 v200, v200\n"
      "v_exp_f32 v201, v201\n"
      "v_exp_f32 v202, v202\n"
      "v_exp_f32 v203, v203\n"
      "v_smfmac_f32_32x32x32_bf16 v[16:31], v[212:215], v[192:199], v236\n"
      "buffer_load_dwordx4 v[230:233], %[vv], %[rs], %[so] offen offset:1024 sc1\n"
      "v_exp_f32 v204, v204\n"
      "v_exp_f32 v205, v205\n"
      "v_exp_f32 v206, v206\n"
      "v_exp_f32 v207, v207\n"
      "s_add_u32 %[so], %[so], 5120\n"
      "v_exp_f32 v176, v176\n"
      "v_exp_f32 v177, v177\n"
      "v_smfmac_f32_32x32x32_bf16 v[16:31], v[216:219], v[200:207], v236\n"
      "v_exp_f32 v178, v178\n"
      "v_exp_f32 v179, v179\n"
      "v_exp_f32 v180, v180\n"
      "v_mfma_f32_32x32x16_bf16 v[192:207], v[208:211], v[140:143], 0\n"
      "v_exp_f32 v181, v181\n"
      "v_exp_f32 v182, v182\n"
      "v_exp_f32 v183, v183\n"
      "v_mfma_f32_32x32x8_bf16 v[192:207], v[220:221], v[166:167], v[192:207]\n"
      "v_exp_f32 v184, v184\n"
      "v_exp_f32 v185, v185\n"
      "v_exp_f32 v186, v186\n"
      "v_exp_f32 v187, v187\n"
      "v_smfmac_f32_32x32x32_bf16 v[32:47], v[212:215], v[176:183], v236\n"
      "v_exp_f32 v188, v188\n"
      "v_exp_f32 v189, v189\n"
      "v_exp_f32 v190, v190\n"
      "v_exp_f32 v191, v191\n"
      "v_exp_f32 v192, v192\n"
      "v_exp_f32 v193, v193\n"
      "v_smfmac_f32_32x32x32_bf16 v[32:47], v[216:219], v[184:191], v236\n"
      "v_exp_f32 v194, v194\n"
      "v_exp_f32 v195, v195\n"
      "v_exp_f32 v196, v196\n"
      "v_mfma_f32_32x32x16_bf16 v[176:191], v[208:211], v[144:147], 0\n"
      "v_exp_f32 v197, v197\n"
      "v_exp_f32 v198, v198\n"
      "v_exp_f32 v199, v199\n"
      "v_mfma_f32_32x32x8_bf16 v[176:191], v[220:221], v[168:169], v[176:191]\n"
      "v_exp_f32 v200, v200\n"
      "v_exp_f32 v201, v201\n"
      "v_exp_f32 v202, v202\n"
      "v_exp_f32 v203, v203\n"
      "v_smfmac_f32_32x32x32_bf16 v[48:63], v[212:215], v[192:199], v236\n"
      "v_exp_f32 v204, v204\n"
      "v_exp_f32 v205, v205\n"
      "v_exp_f32 v206, v206\n"
      "v_exp_f32 v207, v207\n"
      "v_exp_f32 v176, v176\n"
      "v_exp_f32 v177, v177\n"
      "v_smfmac_f32_32x32x32_bf16 v[48:63], v[216:219], v[200:207], v236\n"
      "v_exp_f32 v178, v178\n"
      "v_exp_f32 v179, v179\n"
      "v_exp_f32 v180, v180\n"
      "v_mfma_f32_32x32x16_bf16 v[192:207], v[208:211], v[148:151], 0\n"
      "v_exp_f32 v181, v181\n"
      "v_exp_f32 v182, v182\n"
      "v_exp_f32 v183, v183\n"
      "v_mfma_f32_32x32x8_bf16 v[192:207], v[220:221], v[170:171], v[192:207]\n"
      "v_exp_f32 v184, v184\n"
      "v_exp_f32 v185, v185\n"
      "v_exp_f32 v186, v186\n"
      "v_exp_f32 v187, v187\n"
      "v_smfmac_f32_32x32x32_bf16 v[64:79], v[212:215], v[176:183], v236\n"
      "v_exp_f32 v188, v188\n"
      "v_exp_f32 v189, v189\n"
      "v_exp_f32 v190, v190\n"
      "v_exp_f32 v191, v191\n"
      "v_exp_f32 v192, v192\n"
      "v_exp_f32 v193, v193\n"
      "v_smfmac_f32_32x32x32_bf16 v[64:79], v[216:219], v[184:191], v236\n"
      "v_exp_f32 v194, v194\n"
      "v_exp_f32 v195, v195\n"
      "v_exp_f32 v196, v196\n"
      "v_mfma_f32_32x32x16_bf16 v[176:191], v[208:211], v[152:155], 0\n"
      "v_exp_f32 v197, v197\n"
      "v_exp_f32 v198, v198\n"
      "v_exp_f32 v199, v199\n"
      "v_mfma_f32_32x32x8_bf16 v[176:191], v[220:221], v[172:173], v[176:191]\n"
      "v_exp_f32 v200, v200\n"
      "v_exp_f32 v201, v201\n"
      "v_exp_f32 v202, v202\n"
      "v_exp_f32 v203, v203\n"
      "v_smfmac_f32_32x32x32_bf16 v[80:95], v[212:215], v[192:199], v236\n"
      "v_exp_f32 v204, v204\n"
      "v_exp_f32 v205, v205\n"
      "v_exp_f32 v206, v206\n"
      "v_exp_f32 v207, v207\n"
      "v_exp_f32 v176, v176\n"
      "v_exp_f32 v177, v177\n"
      "v_smfmac_f32_32x32x32_bf16 v[80:95], v[216:219], v[200:207], v236\n"
      "v_exp_f32 v178, v178\n"
      "v_exp_f32 v179, v179\n"
      "v_exp_f32 v180, v180\n"
      "v_mfma_f32_32x32x16_bf16 v[192:207], v[208:211], v[156:159], 0\n"
      "v_exp_f32 v181, v181\n"
      "v_exp_f32 v182, v182\n"
      "v_exp_f32 v183, v183\n"
      "v_mfma_f32_32x32x8_bf16 v[192:207], v[220:221], v[174:175], v[192:207]\n"
      "v_exp_f32 v184, v184\n"
      "v_exp_f32 v185, v185\n"
      "v_exp_f32 v186, v186\n"
      "v_exp_f32 v187, v187\n"
      "v_smfmac_f32_32x32x32_bf16 v[96:111], v[212:215], v[176:183], v236\n"
      "v_exp_f32 v188, v188\n"
      "v_exp_f32 v189, v189\n"
      "v_exp_f32 v190, v190\n"
      "v_exp_f32 v191, v191\n"
      "v_exp_f32 v192, v192\n"
      "v_exp_f32 v193, v193\n"
      "v_smfmac_f32_32x32x32_bf16 v[96:111], v[216:219], v[184:191], v236\n"
      "v_exp_f32 v194, v194\n"
      "v_exp_f32 v195, v195\n"
      "v_exp_f32 v196, v196\n"
      "s_waitcnt vmcnt(0)\n"
      "v_mfma_f32_32x32x16_bf16 v[176:191], v[222:225], v[128:131], 0\n"
      "v_exp_f32 v197, v197\n"
      "v_exp_f32 v198, v198\n"
      "v_exp_f32 v199, v199\n"
      "v_mfma_f32_32x32x8_bf16 v[176:191], v[234:235], v[160:161], v[176:191]\n"
      "v_exp_f32 v200, v200\n"
      "v_exp_f32 v201, v201\n"
      "v_exp_f32 v202, v202\n"
      "v_exp_f32 v203, v203\n"
      "v_smfmac_f32_32x32x32_bf16 v[112:127], v[212:215], v[192:199], v236\n"
      "v_exp_f32 v204, v204\n"
      "v_exp_f32 v205, v205\n"
      "v_exp_f32 v206, v206\n"
      "v_exp_f32 v207, v207\n"
      "v_exp_f32 v176, v176\n"
      "v_exp_f32 v177, v177\n"
      "v_smfmac_f32_32x32x32_bf16 v[112:127], v[216:219], v[200:207], v236\n"
      "v_exp_f32 v178, v178\n"
      "v_exp_f32 v179, v179\n"
      "v_exp_f32 v180, v180\n"
      "v_mfma_f32_32x32x16_bf16 v[192:207], v[222:225], v[132:135], 0\n"
      "v_exp_f32 v181, v181\n"
      "v_exp_f32 v182, v182\n"
      "v_exp_f32 v183, v183\n"
      "v_mfma_f32_32x32x8_bf16 v[192:207], v[234:235], v[162:163], v[192:207]\n"
      "v_exp_f32 v184, v184\n"
      "v_exp_f32 v185, v185\n"
      "v_exp_f32 v186, v186\n"
      "v_exp_f32 v187, v187\n"
      "v_smfmac_f32_32x32x32_bf16 v[0:15], v[226:229], v[176:183], v236\n"
      "v_exp_f32 v188, v188\n"
      "v_exp_f32 v189, v189\n"
      "v_exp_f32 v190, v190\n"
      "v_exp_f32 v191, v191\n"
      "v_exp_f32 v192, v192\n"
      "v_exp_f32 v193, v193\n"
      "v_smfmac_f32_32x32x32_bf16 v[0:15], v[230:233], v[184:191], v236\n"
      "buffer_load_dwordx4 v[208:211], %[vk0], %[rs], %[so] offen sc1\n"
      "v_exp_f32 v194, v194\n"
      "v_exp_f32 v195, v195\n"
      "v_exp_f32 v196, v196\n"
      "v_mfma_f32_32x32x16_bf16 v[176:191], v[222:225], v[136:139], 0\n"
      "buffer_load_dwordx2 v[220:221], %[vk1], %[rs], %[so] offen sc1\n"
      "v_exp_f32 v197, v197\n"
      "v_exp_f32 v198, v198\n"
      "v_exp_f32 v199, v199\n"
      "v_mfma_f32_32x32x8_bf16 v[176:191], v[234:235], v[164:165], v[176:191]\n"
      "buffer_load_dwordx4 v[212:215], %[vv], %[rs], %[so] offen sc1\n"
      "v_exp_f32 v200, v200\n"
      "v_exp_f32 v201, v201\n"
      "v_exp_f32 v202, v202\n"
      "v_exp_f32 v203, v203\n"
      "v_smfmac_f32_32x32x32_bf16 v[16:31], v[226:229], v[192:199], v236\n"
      "buffer_load_dwordx4 v[216:219], %[vv], %[rs], %[so] offen offset:1024 sc1\n"
      "v_exp_f32 v204, v204\n"
      "v_exp_f32 v205, v205\n"
      "v_exp_f32 v206, v206\n"
      "v_exp_f32 v207, v207\n"
      "s_add_u32 %[so], %[so], 5120\n"
      "v_exp_f32 v176, v176\n"
      "v_exp_f32 v177, v177\n"
      "v_smfmac_f32_32x32x32_bf16 v[16:31], v[230:233], v[200:207], v236\n"
      "v_exp_f32 v178, v178\n"
      "v_exp_f32 v179, v179\n"
      "v_exp_f32 v180, v180\n"
      "v_mfma_f32_32x32x16_bf16 v[192:207], v[222:225], v[140:143], 0\n"
      "v_exp_f32 v181, v181\n"
      "v_exp_f32 v182, v182\n"
      "v_exp_f32 v183, v183\n"
      "v_mfma_f32_32x32x8_bf16 v[192:207], v[234:235], v[166:167], v[192:207]\n"
      "v_exp_f32 v184, v184\n"
      "v_exp_f32 v185, v185\n"
      "v_exp_f32 v186, v186\n"
      "v_exp_f32 v187, v187\n"
      "v_smfmac_f32_32x32x32_bf16 v[32:47], v[226:229], v[176:183], v236\n"
      "v_exp_f32 v188, v188\n"
      "v_exp_f32 v189, v189\n"
      "v_exp_f32 v190, v190\n"
      "v_exp_f32 v191, v191\n"
      "v_exp_f32 v192, v192\n"
      "v_exp_f32 v193, v193\n"
      "v_smfmac_f32_32x32x32_bf16 v[32:47], v[230:233], v[184:191], v236\n"
      "v_exp_f32 v194, v194\n"
      "v_exp_f32 v195, v195\n"
      "v_exp_f32 v196, v196\n"
      "v_mfma_f32_32x32x16_bf16 v[176:191], v[222:225], v[144:147], 0\n"
      "v_exp_f32 v197, v197\n"
      "v_exp_f32 v198, v198\n"
      "v_exp_f32 v199, v199\n"
      "v_mfma_f32_32x32x8_bf16 v[176:191], v[234:235], v[168:169], v[176:191]\n"
      "v_exp_f32 v200, v200\n"
      "v_exp_f32 v201, v201\n"
      "v_exp_f32 v202, v202\n"
      "v_exp_f32 v203, v203\n"
      "v_smfmac_f32_32x32x32_bf16 v[48:63], v[226:229], v[192:199], v236\n"
      "v_exp_f32 v204, v204\n"
      "v_exp_f32 v205, v205\n"
      "v_exp_f32 v206, v206\n"
      "v_exp_f32 v207, v207\n"
      "v_exp_f32 v176, v176\n"
      "v_exp_f32 v177, v177\n"
      "v_smfmac_f32_32x32x32_bf16 v[48:63], v[230:233], v[200:207], v236\n"
      "v_exp_f32 v178, v178\n"
      "v_exp_f32 v179, v179\n"
      "v_exp_f32 v180, v180\n"
      "v_mfma_f32_32x32x16_bf16 v[192:207], v[222:225], v[148:151], 0\n"
      "v_exp_f32 v181, v181\n"
      "v_exp_f32 v182, v182\n"
      "v_exp_f32 v183, v183\n"
      "v_mfma_f32_32x32x8_bf16 v[192:207], v[234:235], v[170:171], v[192:207]\n"
      "v_exp_f32 v184, v184\n"
      "v_exp_f32 v185, v185\n"
      "v_exp_f32 v186, v186\n"
      "v_exp_f32 v187, v187\n"
      "v_smfmac_f32_32x32x32_bf16 v[64:79], v[226:229], v[176:183], v236\n"
      "v_exp_f32 v188, v188\n"
      "v_exp_f32 v189, v189\n"
      "v_exp_f32 v190, v190\n"
      "v_exp_f32 v191, v191\n"
      "v_exp_f32 v192, v192\n"
      "v_exp_f32 v193, v193\n"
      "v_smfmac_f32_32x32x32_bf16 v[64:79], v[230:233], v[184:191], v236\n"
      "v_exp_f32 v194, v194\n"
      "v_exp_f32 v195, v195\n"
      "v_exp_f32 v196, v196\n"
      "v_mfma_f32_32x32x16_bf16 v[176:191], v[222:225], v[152:155], 0\n"
      "v_exp_f32 v197, v197\n"
      "v_exp_f32 v198, v198\n"
      "v_exp_f32 v199, v199\n"
      "v_mfma_f32_32x32x8_bf16 v[176:191], v[234:235], v[172:173], v[176:191]\n"
      "v_exp_f32 v200, v200\n"
      "v_exp_f32 v201, v201\n"
      "v_exp_f32 v202, v202\n"
      "v_exp_f32 v203, v203\n"
      "v_smfmac_f32_32x32x32_bf16 v[80:95], v[226:229], v[192:199], v236\n"
      "v_exp_f32 v204, v204\n"
      "v_exp_f32 v205, v205\n"
      "v_exp_f32 v206, v206\n"
      "v_exp_f32 v207, v207\n"
      "v_exp_f32 v176, v176\n"
      "v_exp_f32 v177, v177\n"
      "v_smfmac_f32_32x32x32_bf16 v[80:95], v[230:233], v[200:207], v236\n"
      "v_exp_f32 v178, v178\n"
      "v_exp_f32 v179, v179\n"
      "v_exp_f32 v180, v180\n"
      "v_mfma_f32_32x32x16_bf16 v[192:207], v[222:225], v[156:159], 0\n"
      "v_exp_f32 v181, v181\n"
      "v_exp_f32 v182, v182\n"
      "v_exp_f32 v183, v183\n"
      "v_mfma_f32_32x32x8_bf16 v[192:207], v[234:235], v[174:175], v[192:207]\n"
      "v_exp_f32 v184, v184\n"
      "v_exp_f32 v185, v185\n"
      "v_exp_f32 v186, v186\n"
      "v_exp_f32 v187, v187\n"
      "v_smfmac_f32_32x32x32_bf16 v[96:111], v[226:229], v[176:183], v236\n"
      "v_exp_f32 v188, v188\n"
      "v_exp_f32 v189, v189\n"
      "v_exp_f32 v190, v190\n"
      "v_exp_f32 v191, v191\n"
      "v_exp_f32 v192, v192\n"
      "v_exp_f32 v193, v193\n"
      "v_smfmac_f32_32x32x32_bf16 v[96:111], v[230:233], v[184:191], v236\n"
      "v_exp_f32 v194, v194\n"
      "v_exp_f32 v195, v195\n"
      "v_exp_f32 v196, v196\n"
      "s_waitcnt vmcnt(0)\n"
      "v_mfma_f32_32x32x16_bf16 v[176:191], v[208:211], v[128:131], 0\n"
      "v_exp_f32 v197, v197\n"
      "v_exp_f32 v198, v198\n"
      "v_exp_f32 v199, v199\n"
      "v_mfma_f32_32x32x8_bf16 v[176:191], v[220:221], v[160:161], v[176:191]\n"
      "v_exp_f32 v200, v200\n"
      "v_exp_f32 v201, v201\n"
      "v_exp_f32 v202, v202\n"
      "v_exp_f32 v203, v203\n"
      "v_smfmac_f32_32x32x32_bf16 v[112:127], v[226:229], v[192:199], v236\n"
      "v_exp_f32 v204, v204\n"
      "v_exp_f32 v205, v205\n"
      "v_exp_f32 v206, v206\n"
      "v_exp_f32 v207, v207\n"
      "s_sub_u32 %[cnt], %[cnt], 1\n"
      "s_cmp_lg_u32 %[cnt], 0\n"
      "s_cbranch_scc1 1b\n"
      "s_nop 1\n"
      "v_smfmac_f32_32x32x32_bf16 v[112:127], v[230:233], v[200:207], v236\n"
      "s_nop 15\n"
      "s_waitcnt vmcnt(0)\n"
                    : "=&{v[0:31]}"(o0), "=&{v[32:63]}"(o1), "=&{v[64:95]}"(o2), "=&{v[96:127]}"(o3), [so] "+s"(so), [cnt] "+s"(cnt)
                    : "{v[128:159]}"(q0), "{v[160:175]}"(q1), [vk0] "v"(vk0), [vk1] "v"(vk1), [vv] "v"(vv), [rs] "s"(ws)
                    : "v176", "v177", "v178", "v179", "v180", "v181", "v182", "v183", "v184", "v185", "v186", "v187", "v188", "v189", "v190", "v191", "v192", "v193", "v194", "v195", "v196", "v197", "v198", "v199", "v200", "v201", "v202", "v203", "v204", "v205", "v206", "v207", "v208", "v209", "v210", "v211", "v212", "v213", "v214", "v215", "v216", "v217", "v218", "v219", "v220", "v221", "v222", "v223", "v224", "v225", "v226", "v227", "v228", "v229", "v230", "v231", "v232", "v233", "v234", "v235", "v236", "scc", "memory");
                STAMP(7);
                auto put = [&](const f32x32& o, int d) {
                    const u32 a = lds0 + (((w + d) & 3) * 3 + (d - 1)) * 6144 + lane * 16;
#pragma unroll
                    for (int t = 0; t < 2; ++t)
#pragma unroll
                        for (int g = 0; g < 3; ++g) *(LAS f32x4*)(a + (t * 3 + g) * 1024) = f32x4{o[16 * t + 4 * g], o[16 * t + 4 * g + 1], o[16 * t + 4 * g + 2], o[16 * t + 4 * g + 3]};
                };
                put(o1, 1); put(o2, 2); put(o3, 3);
                wait_bar<63>();
                u32 pkd[2][6];
#pragma unroll
                for (int t = 0; t < 2; ++t) {
                    f32x4 v[3];
#pragma unroll
                    for (int g = 0; g < 3; ++g) {
                        v[g] = f32x4{o0[16 * t + 4 * g], o0[16 * t + 4 * g + 1], o0[16 * t + 4 * g + 2], o0[16 * t + 4 * g + 3]};
#pragma unroll
                        for (int d = 0; d < 3; ++d) v[g] += *(const LAS f32x4*)(lds0 + (w * 3 + d) * 6144 + (t * 3 + g) * 1024 + lane * 16);
                    }
                    auto r = __builtin_amdgcn_permlane32_swap(__float_as_uint(v[2][0]), __float_as_uint(v[2][0]), false, false);
                    const float inv = 1.f / __uint_as_float(r[1]);
#pragma unroll
                    for (int g = 0; g < 3; ++g) { pkd[t][2 * g] = pk2(v[g][0] * inv, v[g][1] * inv); pkd[t][2 * g + 1] = pk2(v[g][2] * inv, v[g][3] * inv); }
                }
                const u32 oso = WS_ACT + (tb * 4 + (w >> 1) * 2) * (296 * 256) + (216 + 10 * hg + 5 * (w & 1)) * 256;
                const u32 ovo = (r32 >> 4) * (296 * 256) + (r32 & 15) * 16;
                st16c(u32x4{pkd[0][0], pkd[0][1], pkd[0][2], pkd[0][3]}, ws, ovo + hh * 256, oso);
                st16c(u32x4{pkd[1][0], pkd[1][1], pkd[1][2], pkd[1][3]}, ws, ovo + hh * 256 + 512, oso);
                if (!hh) st16c(u32x4{pkd[0][4], pkd[0][5], pkd[1][4], pkd[1][5]}, ws, ovo + 1024, oso);
                if constexpr (FUSED) { wait_bar<0>(); if (tid == 0) st8c(u32x2{tag, ~tag}, ws, 0, WS_FLAGC + (tb * 8 + hg) * 8); }
                STAMP(8);
            }
        } else {
            const int d = w - 4;
            const u32 va = lane * 16;
            const u32 sA = WS_WT + hg * 5 * (296 * 256), sB = WS_ACT + tb * 4 * (296 * 256);
            u32x4 fa[3][5], fb[3][4];
            auto ldstep = [&](int slot, int S) {
#pragma unroll
                for (int c = 0; c < 5; ++c) fa[slot][c] = ld16c(ws, va + c * (296 * 256), sA + S * 1024);
#pragma unroll
                for (int t = 0; t < 4; ++t) fb[slot][t] = ld16c(ws, va + t * (296 * 256), sB + S * 1024);
            };
            auto mmstep = [&](int slot) {
#pragma unroll
                for (int c = 0; c < 5; ++c)
#pragma unroll
                    for (int t = 0; t < 4; ++t) acc2[c][t] = mfma_s(fa[slot][c], fb[slot][t], acc2[c][t]);
            };
            if constexpr (MASK & 8) {
#pragma unroll
                for (int c = 0; c < 5; ++c)
#pragma unroll
                    for (int t = 0; t < 4; ++t) acc2[c][t] = f32x4{0.f, 0.f, 0.f, 0.f};
                if constexpr (FUSED) wait_flags<2>(ws, WS_FLAGB, lane * 16, tag);
                STAMP(5);
                ldstep(0, d); ldstep(1, d + 4);
                for (int i = 0; i < 12; i += 3) {
                    ldstep(2, d + 4 * (i + 2)); SBAR(); mmstep(0); SBAR();
                    ldstep(0, d + 4 * (i + 3)); SBAR(); mmstep(1); SBAR();
                    ldstep(1, min(d + 4 * (i + 4), 53)); SBAR(); mmstep(2); SBAR();
                }
                mmstep(0);
                if (d < 2) mmstep(1);
                STAMP(6);
            }
            if constexpr (MASK & 4) { wait_bar<63>(); if constexpr (FUSED) wait_bar<63>(); }
            if constexpr (MASK & 8) {
                if constexpr (FUSED) wait_flags<1>(ws, WS_FLAGC + tb * 64, (lane & 3) * 16, tag);
                STAMP(7);
                const int S0 = 54 + d * 5;
                ldstep(0, S0); ldstep(1, S0 + 1); ldstep(2, S0 + 2); SBAR();
                mmstep(0); SBAR(); ldstep(0, S0 + 3); SBAR();
                mmstep(1); SBAR(); ldstep(1, S0 + 4); SBAR();
                mmstep(2); mmstep(0); mmstep(1);
                STAMP(8);
#pragma unroll
                for (int c = 0; c < 5; ++c)
#pragma unroll
                    for (int t = 0; t < 4; ++t) *(LAS f32x4*)(lds0 + d * 21504 + (t * 16 + (lane & 15)) * 336 + c * 64 + (lane >> 4) * 16) = acc2[c][t];
            }
        }
        if constexpr (MASK & 8) {
            wait_bar<63>();
            for (int c = tid; c < 640; c += 512) {
                const int tok = c / 10, ch = c - tok * 10;
                const u32 go = (tb * 64 + tok) * 1280 + hg * 160 + ch * 16;
                const u32x4 xv = __builtin_amdgcn_raw_buffer_load_b128(rsx, go, 0, 0);
                f32x4 s0 = {bflo(xv[0]), bfhi(xv[0]), bflo(xv[1]), bfhi(xv[1])}, s1 = {bflo(xv[2]), bfhi(xv[2]), bflo(xv[3]), bfhi(xv[3])};
#pragma unroll
                for (int p = 0; p < 4; ++p) { const u32 a = lds0 + p * 21504 + tok * 336 + ch * 32; s0 += *(const LAS f32x4*)a; s1 += *(const LAS f32x4*)(a + 16); }
                *(u32x4*)((char*)out + go) = u32x4{pk2(s0[0], s0[1]), pk2(s0[2], s0[3]), pk2(s1[0], s1[1]), pk2(s1[2], s1[3])};
            }
            if constexpr (FUSED) { if (b == 0 && tid == 0) st4c(tag, ws, 0, WS_EPOCH); }
            STAMP(9);
        }
    }
    STAMP_FLUSH();
}

extern "C" void kernel_launch(void* const* d_in, const int* in_sizes, int n_in, void* d_out, int out_size, void* d_ws, size_t ws_size, hipStream_t stream) {
    const u16 *x = (const u16*)d_in[0], *wq = (const u16*)d_in[1], *wk = (const u16*)d_in[2], *wv = (const u16*)d_in[3], *wo = (const u16*)d_in[4];
    const u16 *wg = (const u16*)d_in[6], *wu = (const u16*)d_in[7], *wd = (const u16*)d_in[8];
    fused_kernel<15><<<256, 512, 0, stream>>>(x, wq, wk, wv, wo, wg, wu, wd, (u16*)d_out, (char*)d_ws);
}
```

```cpp
#include <hip/hip_runtime.h>
#include <hip/hip_bf16.h>
#include <utility>

typedef unsigned short u16;
typedef unsigned int u32;
typedef unsigned long long u64;
using f32x4 = __attribute__((ext_vector_type(4))) float;
using f32x16 = __attribute__((ext_vector_type(16))) float;
using f32x32 = __attribute__((ext_vector_type(32))) float;
using u32x2 = __attribute__((ext_vector_type(2))) u32;
using u32x4 = __attribute__((ext_vector_type(4))) u32;
using u32x8 = __attribute__((ext_vector_type(8))) u32;
using u32x16 = __attribute__((ext_vector_type(16))) u32;
using u32x32 = __attribute__((ext_vector_type(32))) u32;
using bf16x2 = __attribute__((ext_vector_type(2))) __bf16;
using bf16x8 = __attribute__((ext_vector_type(8))) __bf16;
using s16x4 = __attribute__((ext_vector_type(4))) short;
using f32x2 = __attribute__((ext_vector_type(2))) float;
typedef __amdgpu_buffer_rsrc_t rsrc_t;
#define DI __device__ __forceinline__
#define LAS __attribute__((address_space(3)))
#define AI __attribute__((always_inline))

constexpr int S_ = 2048, D_ = 640, F_ = 1728;
constexpr int NCH = 300;
constexpr u32 TS = NCH * 256;
constexpr u32 WS_EPOCH = 0;
constexpr u32 WS_FLAGQ = 4096, WS_FLAGC = 12288;
constexpr u32 WS_KMAX = 20480;
constexpr u32 WS_STAT = 24576;
constexpr u32 WS_CS = WS_STAT + 32768;
constexpr u32 WS_HINT = WS_CS + 32 * 4096;
constexpr u32 WS_FLAGA = WS_HINT + 65536, WS_FLAGW = WS_FLAGA + 32768;
constexpr u32 WS_KV = 4096 + 256 * 1280;
constexpr u32 WS_Q = WS_KV + 128 * 2560;
constexpr u32 WS_ACT = WS_Q + 64 * 80 * 512;
constexpr u32 WS_WT = WS_ACT + 128 * TS;
constexpr u32 WS_DBG = WS_WT + 40 * TS;
static_assert(WS_FLAGW + 32768 <= WS_KV, "");
constexpr u32 WS_END = WS_DBG + 262144 + 524288;
static_assert(WS_KV % 1024 == 0 && WS_Q % 1024 == 0 && WS_ACT % 1024 == 0 && WS_WT % 256 == 0, "align");
constexpr u32 L_SCR = 0, L_RING = 7168, L_WKV = L_RING + 131072, L_TOTAL = 163840;
constexpr u32 L_PROG = 0, L_DRAIN = 16, L_QSIG = 32, L_GO = 48, L_QGO = 52, L_CK = 64, L_ONE = 128, L_ZERO = 144, L_ATT = 4096, L_AW = 39936, L_FINO = 18432;
constexpr u32 L_QST = L_RING + 6 * 16384, L_KST = L_QST + 12288;
constexpr int AUX_SC1 = 16;
constexpr u32 SPIN_CAP = 1u << 17;

template <int N> struct IC { static constexpr int value = N; };
template <typename Fn, int... I> DI void sfor_impl(Fn&& f, std::integer_sequence<int, I...>) { (f(IC<I>{}), ...); }
template <int N, typename Fn> DI void sfor(Fn&& f) { sfor_impl(f, std::make_integer_sequence<int, N>{}); }

DI rsrc_t mkrs(const void* p, u32 bytes) { return __builtin_amdgcn_make_buffer_rsrc((void*)p, (short)0, (int)bytes, 0x00020000); }
DI float bflo(u32 v) { return __uint_as_float(v << 16); }
DI float bfhi(u32 v) { return __uint_as_float(v & 0xffff0000u); }
DI u32 pk2(float a, float b) { f32x2 v = {a, b}; return __builtin_bit_cast(u32, __builtin_convertvector(v, bf16x2)); }
DI float relu(float a) { return __int_as_float(max(__float_as_int(a), 0)); }
extern "C" __device__ float fmul(float, float) __asm("llvm.amdgcn.fmul.legacy");
DI float ffma(float a, float b, float c) { float r; asm("v_fma_f32 %0, %1, %2, %3" : "=v"(r) : "v"(a), "v"(b), "v"(c)); return r; }
DI float fadd(float a, float b) { float r; asm("v_add_f32 %0, %1, %2" : "=v"(r) : "v"(a), "v"(b)); return r; }
DI float rbf(float a) { return bflo(pk2(a, 0.f)); }
template <int C> DI float dpp_add(float v) { return v + __uint_as_float(__builtin_amdgcn_update_dpp(0u, __float_as_uint(v), C, 0xf, 0xf, true)); }
DI float x16_add(float v) { auto r = __builtin_amdgcn_permlane16_swap(__float_as_uint(v), __float_as_uint(v), false, false); return __uint_as_float(r[0]) + __uint_as_float(r[1]); }
DI float x32_add(float v) { auto r = __builtin_amdgcn_permlane32_swap(__float_as_uint(v), __float_as_uint(v), false, false); return __uint_as_float(r[0]) + __uint_as_float(r[1]); }
DI float x32_max(float v) { auto r = __builtin_amdgcn_permlane32_swap(__float_as_uint(v), __float_as_uint(v), false, false); return __builtin_fmaxf(__uint_as_float(r[0]), __uint_as_float(r[1])); }
template <int C> DI u32 dpp_maxu(u32 v) { return max(v, (u32)__builtin_amdgcn_update_dpp(0u, v, C, 0xf, 0xf, true)); }
DI float wave_maxu(u32 v) {
    v = dpp_maxu<0x128>(v); v = dpp_maxu<0x124>(v); v = dpp_maxu<0x122>(v); v = dpp_maxu<0x121>(v);
    auto r = __builtin_amdgcn_permlane16_swap(v, v, false, false); v = max(r[0], r[1]);
    r = __builtin_amdgcn_permlane32_swap(v, v, false, false); return __uint_as_float(max(r[0], r[1]));
}
DI f32x16 mfma16(u32x4 a, u32x4 b, f32x16 c) { return __builtin_amdgcn_mfma_f32_32x32x16_bf16(__builtin_bit_cast(bf16x8, a), __builtin_bit_cast(bf16x8, b), c, 0, 0, 0); }
DI f32x16 mfma8(u32x2 a, u32x2 b, f32x16 c) { return __builtin_amdgcn_mfma_f32_32x32x8bf16_1k(__builtin_bit_cast(s16x4, a), __builtin_bit_cast(s16x4, b), c, 0, 0, 0); }
DI f32x4 mfma_s(u32x4 a, u32x4 b, f32x4 c) { return __builtin_amdgcn_mfma_f32_16x16x32_bf16(__builtin_bit_cast(bf16x8, a), __builtin_bit_cast(bf16x8, b), c, 0, 0, 0); }
typedef short v4i16_t __attribute__((ext_vector_type(4)));
DI u32x2 lds_trp(LAS char* a) { return __builtin_bit_cast(u32x2, __builtin_amdgcn_ds_read_tr16_b64_v4i16((LAS v4i16_t*)a)); }
DI u32x4 lds128(u32 a) { return *(const LAS u32x4*)a; }
template <int IMM, int OFF = 0> DI void glds(u32 ldsbase, u32 voff, rsrc_t rs, u32 soff) {
    asm volatile("s_add_u32 m0, %0, %c4\n\ts_nop 0\n\tbuffer_load_dwordx4 %1, %2, %3 offen offset:%c5 lds" ::"s"(ldsbase), "v"(voff), "s"(rs), "s"(soff), "i"(IMM - OFF), "i"(OFF) : "scc", "memory");
}
template <int IMM, int OFF> DI void gldsc(u32 ldsbase, u32 voff, rsrc_t rs, u32 soff) {
    asm volatile("s_add_u32 m0, %0, %c4\n\ts_nop 0\n\tbuffer_load_dwordx4 %1, %2, %3 offen offset:%c5 sc1 lds" ::"s"(ldsbase), "v"(voff), "s"(rs), "s"(soff), "i"(IMM - OFF), "i"(OFF) : "scc", "memory");
}
DI void glds_c(u32 ldsbase, u32 voff, rsrc_t rs, u32 soff) {
    asm volatile("s_mov_b32 m0, %0\n\ts_nop 0\n\tbuffer_load_dwordx4 %1, %2, %3 offen sc1 lds" ::"s"(ldsbase), "v"(voff), "s"(rs), "s"(soff) : "memory");
}
DI int fresh_lane() { int l; asm volatile("v_mbcnt_lo_u32_b32 %0, -1, 0\n\tv_mbcnt_hi_u32_b32 %0, -1, %0" : "=v"(l)); return l; }
template <int N> DI void wait_vm() { asm volatile("s_waitcnt vmcnt(%c0)" ::"i"(N) : "memory"); }
template <int N> DI void wait_bar() { asm volatile("s_waitcnt vmcnt(%c0) lgkmcnt(0)\n\ts_barrier" ::"i"(N) : "memory"); }
#define SBAR() __builtin_amdgcn_sched_barrier(0)
template <int G> DI u32x2 ld8_asm(rsrc_t rs, u32 voff, u32 soff) { u32x2 r; asm volatile("buffer_load_dwordx2 %0, %1, %2, %3 offen ; ALD %c4" : "=v"(r) : "v"(voff), "s"(rs), "s"(soff), "i"(G) : "memory"); return r; }
template <int G, int OFF> DI u32x4 ld16_asm(rsrc_t rs, u32 voff, u32 soff) { u32x4 r; asm volatile("buffer_load_dwordx4 %0, %1, %2, %3 offen offset:%c5 ; ALD %c4" : "=v"(r) : "v"(voff), "s"(rs), "s"(soff), "i"(G), "i"(OFF) : "memory"); return r; }
template <int G> DI u32 ld4c_asm(rsrc_t rs, u32 voff, u32 soff) { u32 r; asm volatile("buffer_load_dword %0, %1, %2, %3 offen sc1 ; ALD %c4" : "=v"(r) : "v"(voff), "s"(rs), "s"(soff), "i"(G) : "memory"); return r; }
template <int G> DI u32x2 ld8c_asm(rsrc_t rs, u32 voff, u32 soff) { u32x2 r; asm volatile("buffer_load_dwordx2 %0, %1, %2, %3 offen sc1 ; ALD %c4" : "=v"(r) : "v"(voff), "s"(rs), "s"(soff), "i"(G) : "memory"); return r; }
template <int G> DI u32x4 ld16c_asm(rsrc_t rs, u32 voff, u32 soff) { u32x4 r; asm volatile("buffer_load_dwordx4 %0, %1, %2, %3 offen sc1 ; ALD %c4" : "=v"(r) : "v"(voff), "s"(rs), "s"(soff), "i"(G) : "memory"); return r; }
DI u32x4 ld16c(rsrc_t rs, u32 voff, u32 soff) { return __builtin_amdgcn_raw_buffer_load_b128(rs, voff, soff, AUX_SC1); }
DI u32x2 ld8c(rsrc_t rs, u32 voff, u32 soff) { return __builtin_amdgcn_raw_buffer_load_b64(rs, voff, soff, AUX_SC1); }
DI u32 ld4c(rsrc_t rs, u32 voff, u32 soff) { return __builtin_amdgcn_raw_buffer_load_b32(rs, voff, soff, AUX_SC1); }
DI void st16c(u32x4 v, rsrc_t rs, u32 voff, u32 soff) { __builtin_amdgcn_raw_buffer_store_b128(v, rs, voff + soff, 0, AUX_SC1); }
DI void st8c(u32x2 v, rsrc_t rs, u32 voff, u32 soff) { __builtin_amdgcn_raw_buffer_store_b64(v, rs, voff, soff, AUX_SC1); }
DI void st4c(u32 v, rsrc_t rs, u32 voff, u32 soff) { __builtin_amdgcn_raw_buffer_store_b32(v, rs, voff, soff, AUX_SC1); }
DI u32x2 split3(float v) {
    const float h = rbf(v), r1 = v - h, m = rbf(r1), r2 = r1 - m;
    return u32x2{pk2(h, m), pk2(r2, 0.f)};
}
DI u32 split2(float v) { const float h = rbf(v); return pk2(h, v - h); }
DI float dot2(u32 a, u32 b, float c) { return __builtin_amdgcn_fdot2_f32_bf16(__builtin_bit_cast(bf16x2, a), __builtin_bit_cast(bf16x2, b), c, false); }
DI u32 tags_bad(const u32x4& f, u32 tag) { u32 r = ((f[0] ^ tag) | (f[2] ^ tag)) | ((f[1] ^ ~tag) | (f[3] ^ ~tag)); asm volatile("" : "+v"(r)); return r; }
DI bool tags_ok(const u32x4& f, u32 tag) { return tags_bad(f, tag) == 0u; }
template <int N, int SLEEP = 3> DI void wait_flags(rsrc_t ws, u32 off, u32 voff, u32 tag) {
#pragma unroll 1
    for (u32 spins = 0; spins < SPIN_CAP; ++spins) {
        u32 bad = 0;
        __builtin_amdgcn_s_sleep(SLEEP);
        asm volatile("" ::: "memory");
#pragma unroll
        for (int i = 0; i < N; ++i) bad |= tags_bad(ld16c(ws, voff + 2048 * i, off), tag);
        if (!__any(bad != 0u)) break;
    }
    asm volatile("" ::: "memory");
}

constexpr int fills_upto(int s) { return s <= 0 ? 0 : (s <= 10 ? 2 * s : s + 10); }
constexpr int free_after(int s) { return 8 + fills_upto(s) > 30 ? 30 : 8 + fills_upto(s); }
constexpr int PRO_PIECES = 8;
constexpr int P0DELAY = 2;
constexpr int KVROLE = 1;
constexpr int KVTG = 1;
constexpr int OUTAUX = 16;
constexpr int N_P0 = 8;
constexpr int N_FLAG = 96;
constexpr int APRIO = 1;
constexpr int DELAY0 = 1;
constexpr int HLATE = 8;
constexpr int HINT_EARLY = 0;
constexpr int SETS = 1;
constexpr int HPOS = 1;
constexpr int SPRIO = 1;
constexpr int WSLEEP = 12;
constexpr int WDELAY = 30;
constexpr int T_ATT = 24000;
constexpr int T_STEP = 1230;
constexpr int N_G2 = 131;
constexpr int HSLEEP = 3;
constexpr int PACE = 5;
constexpr int DPRIO = 1;
constexpr int NDOWN = 12;
constexpr int T_POLL = 60;
constexpr int T_CHECK = 64;
constexpr int T_DMA = 70;
constexpr int T_SET = 62;
constexpr int N_SIG = 158;
constexpr int P3 = 60;
constexpr int LOOK = 10;
constexpr int FAST = 30;
constexpr int RAMP0 = 0;
constexpr int RAMP = 4;
constexpr int LAST_B = 14;
constexpr int FLAG_B = 12;
constexpr int BMASK = 0x57ff;
constexpr bool has_b(int s) { return (BMASK >> s) & 1; }
constexpr int next_b(int s) { int k = s + 1; while (k < 19 && !has_b(k)) ++k; return k; }
constexpr int allowed(int t) { if (t < 2) return 16; int s = (t - 2) / 4; while (!has_b(s)) --s; return 2 * free_after(s + 1); }
constexpr int issued0(int t) {
    if (t < 0) return PRO_PIECES;
    const int p = issued0(t - 1);
    int n = p < 40 ? (p < t + LOOK && t >= RAMP0 && t % RAMP == RAMP - 1 ? p + 2 : p + 1) : ((t & 3) == 1 && t < FAST ? p : p + 1);
    n = n > 60 ? 60 : n;
    return n < allowed(t) ? n : allowed(t);
}
constexpr int issued(int t) { return issued0(t); }
constexpr int GPAT = 1;
constexpr bool group_end(int g) { return GPAT == 0 ? g % 3 == 2 : GPAT == 1 ? (g % 10 == 2 || g % 10 == 5 || g % 10 == 9) : GPAT == 2 ? (g % 15 == 3 || g % 15 == 7 || g % 15 == 11 || g % 15 == 14) : g % GPAT == GPAT - 1; }
constexpr bool skewed(int n) { return (n >> 3) == 9 || (n >> 3) == 10 || (n >> 3) == 19; }
constexpr int slot_t(int n) { return skewed(n) ? (n >> 3) * 4 + (n & 3) : n >> 1; }
constexpr int slot_tile(int n) { return skewed(n) ? (n >> 2) & 1 : n & 1; }
static_assert(has_b(LAST_B) && next_b(LAST_B) == 19 && has_b(7) && has_b(8) && has_b(9) && has_b(FLAG_B), "");
static_assert(issued(4 * LAST_B + 1) == 60, "everything is issued before the last boundary");


template <int wc>
DI void program(const u16* const x, const u16* const wq, const u16* const wk, const u16* const wv, const u16* const wo, const u16* const wg, const u16* const wu, const u16* const wd,
                u16* const out, char* const wsp, const u32 lds0, const int w, const u64 pc, const u32 code_bytes) {
    const int lane = fresh_lane() & 63, b = blockIdx.x;
    const int r32 = lane & 31, hh = lane >> 5;
    const rsrc_t ws = mkrs(wsp, WS_END);
    const rsrc_t rsx = mkrs(x, S_ * D_ * 2);
    u32 ep; asm volatile("s_load_dword %0, %1, 0x0 ; EPLD" : "=s"(ep) : "s"(wsp));
    const int xcd = b & 7, j = b >> 3;
    const int rb = 8 * (xcd >> 2) + (j >> 2), cg = (xcd & 3) + 4 * (j & 3);
    const int tb = xcd * 4 + (j & 3), hg = j >> 2;
    const int tg = w & 3, d = tg, par = hg & 1;
    const int kvtype = cg & 1, kvt = cg >> 1;
    const bool kvwave = tg == KVTG;
    const bool u1q = cg < 5;
    u32 tag;
    rsrc_t rx = rsx, rg, ru, r10, r11;
    u32 vx0, vx1, vw10, vw11, vw20, vw21, st32;
    const u32 ldw = lds0 + tg * 2048, sx = rb * 128 * 1280, sx2 = sx + 64 * 1280;
    auto piece = [&](auto pi) AI {
        constexpr int p = decltype(pi)::value, f = p >> 1, e = p & 1, slot = L_RING + (f & 7) * 16384 + e * 1024;
        if constexpr (f < 20 && (f & 1) == 0) { glds<slot, (f >> 1) * 128>(ldw, e ? vx1 : vx0, rx, sx); glds<slot + 8192, (f >> 1) * 128>(ldw, e ? vx1 : vx0, rx, sx2); }
        else if constexpr (f < 20) { const u32 o = (f - 1) * st32; glds<slot>(ldw, e ? vw11 : vw10, e ? r11 : r10, o); glds<slot + 8192>(ldw, e ? vw11 : vw10, e ? r11 : r10, o + st32); }
        else { const u32 o = (f - 20) * 64 * F_ * 2; glds<slot>(ldw, e ? vw21 : vw20, e ? ru : rg, o); glds<slot + 8192>(ldw, e ? vw21 : vw20, e ? ru : rg, o + 32 * F_ * 2); }
    };
    u32x4 pv[7]; u32x2 pv2; u32 dummy, dummy2, dummy3 = 0;
    const int un = cg + 16 * (rb & 1), chalf = (rb >> 1) & 1, kq = rb >> 2;
    const int wtg = tg * 256 + b;
    const bool wtwave = wtg < 592;
    rsrc_t rwt, rkv; u32 wtgap, wtso;
    if constexpr (wc == 0) {
        vx0 = (16 * tg + (lane >> 3)) * 1280 + (((lane & 7) ^ (lane >> 4)) * 16);
        vx1 = (16 * tg + 8 + (lane >> 3)) * 1280 + (((lane & 7) ^ (4 + (lane >> 4))) * 16);
        asm volatile("" : "+s"(rx));
        sfor<2>(piece);
        SBAR();
        rg = mkrs(wg, D_ * F_ * 2); ru = mkrs(wu, D_ * F_ * 2);
        r10 = u1q ? mkrs(wq, D_ * D_ * 2) : rg; r11 = u1q ? mkrs(wq, D_ * D_ * 2) : ru;
        asm volatile("" : "+s"(rg), "+s"(ru), "+s"(r10), "+s"(r11));
        const u32 st1 = u1q ? D_ * 2 : F_ * 2, c10 = u1q ? cg * 256 : (cg - 5) * 128, c11 = u1q ? c10 + 128 : c10, c2 = (cg + 11) * 128;
        const u32 wsw0 = ((((lane >> 1) & 3) ^ ((lane >> 4) & 1)) * 32) + (lane & 1) * 16, wsw1 = wsw0 ^ 64, wrow = 8 * tg + (lane >> 3);
        vw10 = wrow * st1 + wsw0 + c10; vw11 = wrow * st1 + wsw1 + c11; vw20 = wrow * (F_ * 2) + wsw0 + c2; vw21 = wrow * (F_ * 2) + wsw1 + c2;
        st32 = 32 * st1;
        sfor<PRO_PIECES - 2>([&](auto k) AI { piece(IC<2 + decltype(k)::value>{}); });
    } else {
        {
            asm volatile("global_load_dword %0, %1, %2 ; ALD 5" : "=v"(dummy) : "v"(min((u32)((((j & 7) * 4 + tg) * 12 + min(lane, 11)) * 128), code_bytes - 8)), "s"(pc) : "memory");
        }
        if constexpr (P0DELAY) __builtin_amdgcn_s_sleep(P0DELAY);
        const u32 vs = hh * 1280 + r32 * 16, ss = (rb * 128 + cg * 8 + tg * 2) * 1280;
        pv[5] = ld16_asm<0, 0>(rx, vs, ss); pv[6] = ld16_asm<0, 512>(rx, vs, ss); pv2 = ld8_asm<0>(rx, hh * 1280 + 1024 + r32 * 8, ss);
        const bool isq = un < 5;
        const rsrc_t rc = mkrs(isq ? wq : chalf ? wu : wg, isq ? D_ * D_ * 2 : D_ * F_ * 2);
        const u32 stc = isq ? D_ * 2 : F_ * 2, vo = (lane >> 3) * stc + (lane & 7) * 16 + (isq ? un * 256 + chalf * 128 : (un - 5) * 128);
        sfor<5>([&](auto k) AI { constexpr int i = decltype(k)::value; pv[i] = ld16_asm<0, 0>(rc, vo, (kq * 160 + tg * 40 + 8 * i) * stc); });
        if (tg == 0) dummy3 = ld4c_asm<5>(ws, min(lane, 9) * 128, 4096 + b * 1280);
        SBAR();
        {
            const int X = wtg >> 1, Y = X - 216, y5 = (Y * 205) >> 10, ci = Y - 5 * y5, hb = 8 * (y5 >> 2) + ((y5 >> 1) & 1) + 4 * (y5 & 1);
            const int rowo = hb * 20 + (ci < 4 ? 40 * (ci >> 1) + 4 * (ci & 1) : 16);
            const bool isd = X < 216;
            rwt = mkrs(isd ? wd : wo, !wtwave ? 0 : isd ? F_ * D_ * 2 : D_ * D_ * 2);
            wtgap = (isd ? 4 : ci < 4 ? 8 : 40) * 1280; wtso = (isd ? 8 * X : rowo) * 1280 + (wtg & 1) * 640;
            asm volatile("" : "+s"(rwt), "+s"(wtgap), "+s"(wtso));
        }
        rkv = mkrs(kvtype ? wv : wk, D_ * 40);
        asm volatile("" : "+s"(rkv));
    }
    u32 l16 = lane * 16, sst = tg * 512, skv = kvt * 256, ntag;
    u32 sep0 = u1q ? WS_Q + (rb * 4 + tg) * 40960 + (cg * 16 + 4 * wc) * 512 : WS_ACT + (rb * 8 + tg * 2) * TS + ((cg - 5) * 8 + 4 * wc) * 256;
    u32 sep1 = WS_ACT + (rb * 8 + tg * 2) * TS + ((cg + 11) * 8 + 4 * wc) * 256;
    u32 sg0 = WS_CS + cg * 4096 + tg * 1024, sg1 = WS_STAT + rb * 2048 + (tg & 1) * 1024, lg1 = lds0 + L_SCR + (tg < 2 ? tg * 1024 : 6144);
    u32 sstat = WS_STAT + (rb * 128 + cg * 8 + tg * 2) * 16, scs = WS_CS + un * 4096 + (kq & 1) * 1024 + (kq >> 1) * 512 + chalf * 128;
    u32 kvb = WS_KV + (par * 64 + d * 16) * 2560;
    u32 sqs = WS_Q + (tb * 2 + (tg >> 1)) * 40960 + (4 * (hg >> 1) + 2 * (tg & 1)) * 2560 + par * 1024, ldq = lds0 + tg * 3072, ldk = lds0 + tg * 5120;
    if constexpr (wc == 0) asm volatile("" : "+s"(kvb), "+s"(sqs), "+s"(ldq), "+s"(ldk));
    u32 vfl, sfq = u1q ? WS_FLAGQ + (rb * 2 + (tg >> 1)) * 256 + (cg * 4 + (tg & 1) * 2 + wc) * 8 : 0x7f000000u, sfa = kvwave ? WS_FLAGA + (b >> 4) * 256 + (b & 15) * 8 : 0x7f000000u;
    asm volatile("" : "+s"(sfq), "+s"(sfa));
    u32 skt = WS_KV + (rb * 4 + (kvt >> 1)) * 2560, cgq = cg;
    asm volatile("" : "+v"(l16));
    asm volatile("" : "+s"(sst), "+s"(skv), "+s"(sep0), "+s"(sep1), "+s"(cgq));
    if constexpr (wc == 1) asm volatile("" : "+s"(sg0), "+s"(sg1), "+s"(lg1), "+s"(sstat), "+s"(scs));
    if constexpr (wc == KVROLE) asm volatile("" : "+s"(skt));
    const int tq = (lane >> 2) & 3, tp = lane & 3, tgrp = (lane >> 4) & 1;
    const u32 wa_ = lds0 + L_RING + hh * 2048 + (tp >> 1) * 1024 + tq * 128 + 16 * (tp & 1) + 8 * tgrp;
    const u32 wx_ = (tp & 2) | (tq >> 1);
    u32 wa0 = wa_ + ((2 * wc) ^ wx_) * 32, wa1 = wa_ + ((2 * wc + 1) ^ wx_) * 32;
    u32 wa0h = wa0 + 65536, wa1h = wa1 + 65536; asm volatile("" : "+v"(wa0), "+v"(wa1), "+v"(wa0h), "+v"(wa1h));
    const u32 xa_ = lds0 + L_RING + (tg * 32 + r32) * 128, xsw = (r32 >> 1) & 7;
    u32 ka = lds0 + L_WKV + (32 * (tg >> 1) + 8 * (lane >> 4) + tq) * 40 + (2 * (tp & 1) + (tp >> 1) + (tg & 1)) * 8;
    u32 kxa = lds0 + L_RING + (kvt * 16 + (lane & 15)) * 128 + (((4 * (tg >> 1) + (lane >> 4)) ^ ((lane >> 1) & 7)) * 16), kxh = kxa + 65536;
    if constexpr (wc == 1) asm volatile("" : "+v"(ka), "+v"(kxa), "+v"(kxh));
    constexpr u32 L_CP = L_SCR;
    f32x4 csa = {}; u32x4 sel; float s1, q1;
    auto p0 = [&](auto pi) AI {
        constexpr int part = decltype(pi)::value;
        if constexpr (part == 0) {
            const u32 one = 0x3f80u << (l16 & 16), sd_ = l16 & 96;
            sel = u32x4{sd_ == 0 ? one : 0u, sd_ == 32 ? one : 0u, sd_ == 64 ? one : 0u, sd_ == 96 ? one : 0u};
            asm volatile("" : "+v"(sel));
        }
        if constexpr (part == 1) {
            float s[2] = {0.f, 0.f}, q[2] = {0.f, 0.f};
#pragma unroll
            for (int e = 0; e < 10; ++e) { const u32 v = e < 8 ? pv[5 + (e >> 2)][e & 3] : pv2[e & 1]; s[e & 1] = dot2(v, 0x3f803f80u, s[e & 1]); q[e & 1] = dot2(v, v, q[e & 1]); }
            s1 = s[0] + s[1]; q1 = q[0] + q[1];
        }
        if constexpr (part >= 2 && part < 7) csa = mfma_s(pv[part - 2], sel, csa);
        if constexpr (part == 2) { s1 = dpp_add<0x128>(s1); q1 = dpp_add<0x128>(q1); s1 = dpp_add<0x124>(s1); q1 = dpp_add<0x124>(q1); }
        if constexpr (part == 3) { s1 = dpp_add<0x122>(s1); q1 = dpp_add<0x122>(q1); s1 = dpp_add<0x121>(s1); q1 = dpp_add<0x121>(q1); }
        if constexpr (part == 4) { s1 = fmul(x16_add(s1), 1.f / D_); q1 = x16_add(q1); }
        if constexpr (part == 5) q1 = rbf(rbf(__builtin_fmaxf(ffma(q1, 1.f / D_, -fmul(s1, s1)), 0.f)) + 1e-5f);
        if constexpr (part == 6) q1 = __builtin_amdgcn_rcpf(rbf(__builtin_amdgcn_sqrtf(q1)));
        if constexpr (part >= 1 && part < 7) asm volatile("" : "+v"(s1), "+v"(q1));
        if constexpr (part == 7) {
            *(LAS f32x4*)(lds0 + L_CP + 2 * sst + l16) = csa;
            st16c(u32x4{pk2(-s1, -s1), tag, __float_as_uint(q1), ntag}, ws, (l16 & 511) ? 0x7f000000u : (l16 >> 5) + sstat, 0);
        }
        if constexpr (part == 8) {
            if (tg == 2) {
                const int lane = l16 >> 4;
                f32x2 p[4];
#pragma unroll
                for (int i = 0; i < 4; ++i) { LAS const float* a = (LAS const float*)(lds0 + L_CP + i * 1024 + l16 + ((lane >> 1) & 4)); p[i] = f32x2{a[0], a[2]}; }
                const float c0 = x32_add(fadd(fadd(p[0][0], p[1][0]), fadd(p[2][0], p[3][0]))), c1 = x32_add(fadd(fadd(p[0][1], p[1][1]), fadd(p[2][1], p[3][1])));
                const int c = lane & 15, r = (c & 3) + 4 * (c >> 3) + 16 * ((c >> 2) & 1);
                st16c(u32x4{split2(c0), tag, split2(c1), ntag}, ws, lane < 32 ? (lane >> 4) * 2048 + r * 16 + scs : 0x7f000000u, 0);
            }
        }
    };
    u32x4 xf[40], wf[4];
    f32x16 acc[2]; f32x4 kacc = {}, kcs = {}; u32x4 kx[6], kw, ones;
    float rstd; u32 nm;
    auto rd_frag = [&](auto ni) AI {
        constexpr int n = decltype(ni)::value, t = slot_t(n), hf = slot_tile(n), s = t >> 2, ks = t & 3;
        constexpr int wslot = ((s < 10 ? 2 * s + 1 : s + 10) & 7) * 16384 + ks * 4096;
        if constexpr (n < 160) {
            LAS char* const p = (LAS char*)(wslot < 65536 ? (hf ? wa1 : wa0) : (hf ? wa1h : wa0h)) + (wslot & 65535);
            const u32x2 a0 = lds_trp(p), a1 = lds_trp(p + 512);
            wf[n & 3] = u32x4{a0[0], a0[1], a1[0], a1[1]};
            if constexpr (s < 10 && hf == 0) xf[t] = lds128(xa_ + ((2 * s) & 7) * 16384 + (((2 * ks + hh) ^ xsw) * 16));
        }
    };
    auto gran_bad = [&](const u32x4& g) AI { return (g[1] ^ tag) | (g[3] ^ ntag); };
    u32x4 gst, gc[2], gks; u32 gbad; float krstd, knm;
    LAS char* const pl = (LAS char*)(lds0 + l16);
    auto corr_read = [&](auto ui, auto pi) AI {
        constexpr int u = decltype(ui)::value, part = decltype(pi)::value;
        if constexpr (part == 0) { gc[0] = *(const LAS u32x4*)(pl + L_SCR + 2048 + wc * 2048); gc[1] = *(const LAS u32x4*)(pl + L_SCR + 3072 + wc * 2048); }
        if constexpr (part == 1 && u == 0) gst = lds128(lds0 + L_SCR + ((l16 & 511) | sst));
        if constexpr (part == 2 && u == 0 && wc == 1) gks = lds128(lds0 + L_SCR + ((l16 & 255) | skv));
    };
    auto corr_bad = [&](auto ui, auto pi) AI {
        constexpr int u = decltype(ui)::value, part = decltype(pi)::value;
        if constexpr (part == 0) gbad = gran_bad(gc[0]) | gran_bad(gc[1]);
        if constexpr (part == 1 && u == 0) gbad |= gran_bad(gst);
        if constexpr (part == 2 && u == 0 && wc == 1) gbad |= gran_bad(gks);
        asm volatile("" : "+v"(gbad));
    };
    auto corr_check = [&](auto ui) AI {
        constexpr int u = decltype(ui)::value;
        if (__builtin_expect(__any(gbad != 0u), 0)) {
            u32 spins = 0;
#pragma unroll 1
            do {
                asm volatile("" ::: "memory");
                gc[0] = ld16c(ws, l16, WS_CS + (cg + 16 * u) * 4096 + wc * 2048); gc[1] = ld16c(ws, l16, WS_CS + (cg + 16 * u) * 4096 + wc * 2048 + 1024);
                if constexpr (u == 0) gst = ld16c(ws, l16 & 511, WS_STAT + rb * 2048 + sst);
                if constexpr (u == 0 && wc == 1) gks = ld16c(ws, l16 & 255, WS_STAT + rb * 2048 + skv);
                sfor<3>([&](auto pi) AI { corr_bad(ui, pi); });
            } while (__any(gbad != 0u) && ++spins < SPIN_CAP);
        }
        if constexpr (u == 0) { nm = gst[0]; rstd = __uint_as_float(gst[2]); }
        if constexpr (u == 0 && wc == 1) { krstd = __uint_as_float(gks[2]); knm = bfhi(gks[0]); }
    };
    auto corr_mm = [&](auto ti) AI { constexpr int t = decltype(ti)::value; acc[t] = mfma8(u32x2{gc[0][2 * t], gc[1][2 * t]}, u32x2{nm, nm}, acc[t]); };
    auto kv_x = [&](int buf, auto si) AI { constexpr int o = ((2 * decltype(si)::value) & 7) * 16384; kx[buf] = *(const LAS u32x4*)((LAS char*)(o < 65536 ? kxa : kxh) + (o & 65535)); };
    auto kv_w = [&](auto si) AI { LAS char* const p = (LAS char*)ka + decltype(si)::value * 2560; const u32x2 lo = lds_trp(p), hi = lds_trp(p + 160); kw = u32x4{lo[0], lo[1], hi[0], hi[1]}; };
    constexpr u32 L_KP = L_WKV + 2048;
    auto kv_finish = [&]() AI {
        const int lane = fresh_lane(), g = lane >> 4, key = (kvt & 1) * 16 + (lane & 15);
        f32x4 pp[4];
#pragma unroll
        for (int i = 0; i < 4; ++i) pp[i] = *(const LAS f32x4*)(pl + L_KP + i * 1024);
        float v[8];
#pragma unroll
        for (int e = 0; e < 8; ++e) v[e] = fadd(pp[e >> 2][e & 3], pp[2 + (e >> 2)][e & 3]);
        const u32x4 p = {pk2(v[0], v[1]), pk2(v[2], v[3]), pk2(v[4], v[5]), pk2(v[6], v[7])};
        const u32 kt = skt;
        if (kvtype == 0) {
            st16c(p, ws, (g >> 1) * (64 * 2560) + ((g & 1) * 32 + key) * 16, kt);
            st8c(g ? u32x2{p[2], p[3]} : u32x2{p[0], p[1]}, ws, g == 3 ? key * 8 : g == 0 ? 64 * 2560 + key * 8 : 0x7f000000u, kt + 1024);
            const float lo = ffma(v[0], v[0], ffma(v[1], v[1], ffma(v[2], v[2], fmul(v[3], v[3])))), hi = ffma(v[4], v[4], ffma(v[5], v[5], ffma(v[6], v[6], fmul(v[7], v[7]))));
            u32 n2 = __float_as_uint(x32_add(x16_add((g < 2 ? lo : 0.f) + (g != 2 ? hi : 0.f))));
            n2 = dpp_maxu<0x128>(n2); n2 = dpp_maxu<0x124>(n2); n2 = dpp_maxu<0x122>(n2); n2 = dpp_maxu<0x121>(n2);
            if (lane == 0) st4c(n2, ws, 0, WS_KMAX + (rb * 8 + kvt) * 4);
        } else {
            const u32 vs = lds0 + L_WKV + (8 * (g & 1) + 4 * (g >> 1)) * 48 + (lane & 15) * 2;
#pragma unroll
            for (int e = 0; e < 4; ++e) { *(LAS u16*)(vs + 2 * e * 48) = (u16)p[e]; *(LAS u16*)(vs + (2 * e + 1) * 48) = (u16)(p[e] >> 16); }
            asm volatile("" ::: "memory");
            const u32x4 t = lds128(lds0 + L_WKV + (lane & 31) * 48 + (lane >> 5) * 16);
            const u32 vo = (lane & 31) < 20 ? ((lane >> 5) * 20 + (lane & 31)) * 16 + kt + 1280 + (kvt & 1) * 640 : 0x7f000000u;
            st16c(t, ws, vo, 0); st16c(t, ws, vo, 64 * 2560);
        }
    };
    u32 hp[4], vep, vep1; float esc; u32x4 hold[2][2];
    auto epi_setup = [&](auto ui, auto qi) AI {
        if constexpr (decltype(qi)::value) { vep = l16 + sep0; esc = rstd * (0.22360679774997896f * 1.4426950408889634f); }
        else { vep = ((l16 >> 8) & 1) * TS + ((l16 >> 1) & 256) + (l16 & 255) + (decltype(ui)::value ? sep1 : sep0); esc = rstd * rstd; }
        asm volatile("" : "+v"(vep), "+v"(esc));
    };
    auto epi = [&](auto ui, auto ti, auto ci, auto qi) AI {
        constexpr int u = decltype(ui)::value, t = decltype(ti)::value, c = decltype(ci)::value;
        const f32x16& a = acc[t];
        if constexpr (decltype(qi)::value) {
            constexpr int o = 4 * (c >> 1) + 8 * (c & 1);
            hp[2 * (c & 1)] = pk2(fmul(a[o], esc), fmul(a[o + 1], esc)); hp[2 * (c & 1) + 1] = pk2(fmul(a[o + 2], esc), fmul(a[o + 3], esc));
            if constexpr (c & 1) st16c(u32x4{hp[0], hp[1], hp[2], hp[3]}, ws, vep, ((c >> 1) * 8 + 2 * t) * 512);
            else asm volatile("" : "+v"(hp[0]), "+v"(hp[1]));
        } else {
            constexpr int o = 8 * (c >> 1) + 2 * (c & 1);
            hp[c] = pk2(fmul(fmul(relu(a[o]), esc), a[o + 4]), fmul(fmul(relu(a[o + 1]), esc), a[o + 5]));
            if constexpr (c < 3) asm volatile("" : "+v"(hp[c]));
            else if constexpr (u == 0 || wc == 1) { hold[u][t] = u32x4{hp[0], hp[1], hp[2], hp[3]}; asm volatile("" : "+v"(hold[u][t])); }
            else st16c(u32x4{hp[0], hp[1], hp[2], hp[3]}, ws, vep, 2 * t * 256);
        }
    };
    auto o_addr = [&](int lane) AI { return (u32)(WS_ACT + (tb * 4 + (w & 2)) * TS + (216 + 10 * hg + 5 * (w & 1)) * 256 + ((lane >> 4) & 1) * TS + (lane & 15) * 16 + (lane >> 5) * 256); };
    auto o_store = [&](u32 vo, bool lower, u32x4 a, u32x4 c, u32x4 e) AI {
        st16c(a, ws, vo, 0); st16c(c, ws, vo, 512);
        st16c(e, ws, lower ? vo : 0x7f000000u, 1024);
    };
    auto const_rows = [&](int lane) AI {
        const u32 one = (lane == L_ONE / 16 || lane == (L_ONE + 640) / 16) ? 0x3f803f80u : 0u;
        return lane == L_CK / 16 ? u32x4{0x3f803f80u, 0x00003f80u, 0u, 0u} : u32x4{one, one, one, one};
    };
    constexpr int T_GRAN = 4 * 5 + 2;
    u32x4 fl0, fl1, fl2; u32x2 kmx; u32 qgo;

    u32 aq[4], aqc[4], akv[5], ak0, ak1, av, sk1, sv, aput, apw, srcp[3], vo;
    auto att_setup = [&](auto pi) AI {
        constexpr int part = decltype(pi)::value;
        const int lane = fresh_lane(), r32 = lane & 31, hh = lane >> 5;
        if constexpr (part < 2) {
#pragma unroll
            for (int p = 2 * part; p < 2 * part + 2; ++p) {
                const u32 a = lds0 + L_QST + ((p + tg + 1) & 3) * 3072;
                aq[p] = a + par * 512 + lane * 16; aqc[p] = hh ? lds0 + L_ZERO : a + (par ? 8 : 1024) + lane * 16;
                asm volatile("" : "+v"(aq[p]), "+v"(aqc[p]));
            }
        } else if constexpr (part == 2) {
            const u32 base = lds0 + L_ATT + tg * L_AW;
            ak0 = base + lane * 16; ak1 = hh ? lds0 + L_CK : base + 1024 + lane * 8; av = r32 < 20 ? base + 1280 + (hh * 20 + r32) * 16 : lds0 + (r32 == 20 ? L_ONE : L_ZERO);
            sk1 = hh ? 0u : 5120u; sv = r32 < 20 ? 5120u : 0u;
            asm volatile("" : "+v"(ak0), "+v"(ak1), "+v"(av), "+v"(sk1), "+v"(sv));
        } else if constexpr (part == 3) {
            const u32 dl = L_KST + tg * 5120 - (L_ATT + tg * L_AW);
            akv[0] = ak0 + dl; akv[1] = ak1 + (hh ? 0u : dl); akv[2] = akv[1] + (sk1 >> 1); akv[3] = av + (r32 < 20 ? dl : 0u); akv[4] = akv[3] + (sv >> 1);
            aput = ak0; apw = lds0 + L_PROG + tg * 4;
            asm volatile("" : "+v"(akv[0]), "+v"(akv[1]), "+v"(akv[2]), "+v"(akv[3]), "+v"(akv[4]), "+v"(aput), "+v"(apw));
        } else {
#pragma unroll
            for (int i = 0; i < 3; ++i) srcp[i] = lds0 + L_ATT + ((tg + 3 - i) & 3) * L_AW + i * 6144 + lane * 16;
            vo = o_addr(lane);
            asm volatile("" : "+v"(srcp[0]), "+v"(srcp[1]), "+v"(srcp[2]), "+v"(vo));
        }
    };
    f32x4 acc2[5][4]; u32x4 cvr, unw; u32x2 xv[5]; u32 vwl, vso, vso2, awt, vws[5], vb[4], vxr;
    auto ctile = [&](int c) AI { return c < 4 ? c + par : 4 - 4 * par; };
    auto g2_setup = [&](auto pi) AI {
        constexpr int part = decltype(pi)::value;
        const int lane = l16 >> 4;
        if constexpr (part == 0) { cvr = const_rows(lane); asm volatile("" : "+v"(cvr)); }
        if constexpr (part == 1) { unw = u32x4{~0u, ~0u, ~0u, ~0u}; vwl = ((lane >> 3) & 3) * 1280 + (lane >> 5) * wtgap + (l16 & 112); asm volatile("" : "+v"(unw), "+v"(vwl)); }
        if constexpr (part == 2) { vso = o_addr(lane); asm volatile("" : "+v"(vso)); }
        if constexpr (part == 3) { vso2 = lane < 32 ? vso + 1024 : 0x7f000000u; awt = lds0 + L_ATT + d * L_AW + 6 * 5120 + ((lane >> 2) & 3) * 128 + (lane >> 4) * 32 + (lane & 3) * 8; asm volatile("" : "+v"(vso2), "+v"(awt)); }
        if constexpr (part == 4) {
#pragma unroll
            for (int t = 0; t < 4; ++t) vb[t] = l16 + ((t + d) & 3) * TS;
            asm volatile("" : "+v"(vb[0]), "+v"(vb[1]), "+v"(vb[2]), "+v"(vb[3]));
        }
        if constexpr (part == 5) { vws[0] = (lane >> 4) * TS + (l16 & 255) + (wtwave ? WS_WT + (wtg & 1) * 20 * TS + (wtg >> 1) * 256 : 0x7f000000u); asm volatile("" : "+v"(vws[0])); }
        if constexpr (part == 6) {
#pragma unroll
            for (int it = 1; it < 5; ++it) vws[it] = vws[0] + it * 4 * TS;
            asm volatile("" : "+v"(vws[1]), "+v"(vws[2]), "+v"(vws[3]), "+v"(vws[4]));
        }
        if constexpr (part == 7) { vxr = (tb * 64 + d * 16 + (lane & 15)) * 1280 + hg * 160 + (lane >> 4) * 8; asm volatile("" : "+v"(vxr)); }
        if constexpr (part >= 8) {
            constexpr int c = part - 8;
#pragma unroll
            for (int t = 1; t < 4; ++t) acc2[c][t] = f32x4{0.f, 0.f, 0.f, 0.f};
            asm volatile("" : "+v"(acc2[c][1]), "+v"(acc2[c][2]), "+v"(acc2[c][3]));
        }
    };
    auto slotq = [&](auto ni, auto qi) AI {
        constexpr int n = decltype(ni)::value, t = slot_t(n), tile = slot_tile(n), u = n >> 1, s = n >> 3;
        if constexpr ((n & 7) == 4 && has_b(s)) {
            constexpr int allow = issued(u - 1) - 2 * fills_upto(next_b(s) + 1);
            constexpr int allow2 = allow;
            constexpr int stores = s == FLAG_B ? 1 : (s == 10 && decltype(qi)::value) ? 4 : 0;
            static_assert(allow >= 0 && allow2 >= 0 && (s != 10 || issued(37) >= 2 * fills_upto(next_b(s) + 1)) && (s != FLAG_B || issued(N_FLAG / 2 - 1) >= 2 * fills_upto(next_b(s) + 1)), "");
            if constexpr (wc == 0) wait_bar<2 * (allow2 < allow ? allow2 : allow) + stores>();
            else wait_bar<(s == 8 || s == LAST_B) ? 0 : (s == 2 || s == 3) ? 2 : s == 6 ? 3 : (s >= 4 && s <= 7) ? 1 : 63>();
            SBAR();
        }
        if constexpr (t == 0 || t == 40) acc[tile] = mfma16(wf[n & 3], xf[t % 40], f32x16{}); else acc[tile] = mfma16(wf[n & 3], xf[t % 40], acc[tile]);
        SBAR();
        rd_frag(IC<n + 4>{});
        SBAR();
        if constexpr (wc == 1) {
            constexpr int ks = (n + 4) >> 3, ph = (n + 4) & 7;
            if constexpr (ph == 2 && ks >= 1 && ks <= 7) glds<L_WKV + (ks - 1) * 4096>(lds0 + (ks < 7 ? 2 * sst : 0u), l16, rkv, (ks - 1) * 4096 + (ks < 7 ? 2 * sst : 0u));
            if constexpr (ks < 10 && (n == 0 || (ks > 0 && ph == 0))) kv_x(ks & 3, IC<ks>{});
            if constexpr (n == 19) { ones = u32x4{0x3f803f80u, 0x3f803f80u, 0x3f803f80u, 0x3f803f80u}; asm volatile("" : "+v"(ones)); }
            if constexpr (ks >= 3 && ks < 10) {
                constexpr int s0 = ks == 3 ? 0 : ks == 4 ? 1 : ks == 5 ? 3 : ks == 6 ? 4 : ks == 7 ? 6 : ks;
                constexpr bool more = ks == 4 || ks == 6 || ks == 7;
                if constexpr (ph == 0) kv_w(IC<s0>{});
                if constexpr (ph == 2) kacc = mfma_s(kw, kx[s0 & 3], kacc);
                if constexpr (ph == 3) { kcs = mfma_s(kw, ones, kcs); if constexpr (more) { SBAR(); kv_w(IC<s0 + 1>{}); } }
                if constexpr (ph == 5 && more) kacc = mfma_s(kw, kx[(s0 + 1) & 3], kacc);
                if constexpr (ph == 6 && more) kcs = mfma_s(kw, ones, kcs);
            }
            if constexpr (n == 3) p0(IC<0>{});
            if constexpr (n == N_P0) asm volatile("s_waitcnt vmcnt(1) ; APIN 0\n\t; APIN 5" : "+v"(pv[0]), "+v"(pv[1]), "+v"(pv[2]), "+v"(pv[3]), "+v"(pv[4]), "+v"(pv[5]), "+v"(pv[6]), "+v"(pv2), "+v"(dummy), "+v"(dummy3) :: "memory");
            if constexpr (n >= N_P0 && n < N_P0 + 6) p0(IC<n - N_P0 + 1>{});
            static_assert(N_P0 > 6 && N_P0 + 7 > 14 && N_P0 + 7 < 20 && T_GRAN == 22, "the counts of the boundary waits");
            if constexpr (n == N_P0 + 7) p0(IC<7>{});
            if constexpr (n == 21) p0(IC<8>{});
            if constexpr (n == 74) {
                f32x4 r;
#pragma unroll
                for (int e = 0; e < 4; ++e) r[e] = fmul(fadd(fmul(kcs[e], knm), fmul(kacc[e], 1.f)), krstd);
                *(LAS f32x4*)(lds0 + L_KP + 2 * sst + l16) = r;
            }
            SBAR();
        }
        if constexpr (wc == 0 && n == N_SIG) { asm volatile("s_waitcnt vmcnt(0) ; APIN 4" : "+v"(kmx) :: "memory"); *(volatile LAS u32*)((LAS char*)apw + L_QSIG) = 1u; }
        if constexpr (n == N_FLAG - 1) {
            vfl = l16 == 128 ? sfq : (wc == KVROLE && l16 < 128) ? l16 * 256 + sfa : 0x7f000000u; asm volatile("" : "+v"(vfl));
        }
        if constexpr (n == N_FLAG) {
            static_assert(N_FLAG > 85 && N_FLAG % 2 == 0 && N_FLAG < 8 * FLAG_B + 4, "");
            wait_vm<wc == 0 ? 2 * (issued(u - 1) - issued(40)) : 1>();
            st8c(u32x2{tag, ntag}, ws, vfl, 0);
        }
        if constexpr (wc == KVROLE && n == 76) { if (kvwave) kv_finish(); }
        if constexpr (n == 77) epi_setup(IC<0>{}, qi);
        if constexpr (n == 77) { epi(IC<0>{}, IC<0>{}, IC<0>{}, qi); epi(IC<0>{}, IC<0>{}, IC<1>{}, qi); }
        if constexpr (n == 78 || n == 79) epi(IC<0>{}, IC<0>{}, IC<n - 76>{}, qi);
        if constexpr (n == 81) { epi(IC<0>{}, IC<1>{}, IC<0>{}, qi); epi(IC<0>{}, IC<1>{}, IC<1>{}, qi); }
        if constexpr (n == 82 || n == 83) epi(IC<0>{}, IC<1>{}, IC<n - 80>{}, qi);
        if constexpr (n == 157) { epi(IC<1>{}, IC<0>{}, IC<0>{}, IC<0>{}); epi(IC<1>{}, IC<0>{}, IC<1>{}, IC<0>{}); }
        if constexpr (n == 158 || n == 159) epi(IC<1>{}, IC<0>{}, IC<n - 156>{}, IC<0>{});
        if constexpr (n == 139) { vep1 = cgq < 5 ? 0x7f000000u : vep; asm volatile("" : "+v"(vep1)); }
        if constexpr (wc == 0 && n == 159) { st16c(hold[0][0], ws, vep1, 0); st16c(hold[0][1], ws, vep1, 512); }
        if constexpr (n == 140) epi_setup(IC<1>{}, IC<0>{});
        if constexpr (wc == 1 && n >= N_G2 && n < N_G2 + 26 && (n - N_G2) % 2 == 0) g2_setup(IC<(n - N_G2) / 2>{});
        if constexpr (n >= 61 && n < 64) corr_read(IC<0>{}, IC<n - 61>{});
        if constexpr (n >= 64 && n < 67) corr_bad(IC<0>{}, IC<n - 64>{});
        if constexpr (n == 67) corr_check(IC<0>{});
        if constexpr (n == 68 + wc || n == 70 + wc) corr_mm(IC<(n - 68) / 2>{});
        if constexpr (n == 8 * LAST_B + 5) corr_read(IC<1>{}, IC<0>{});
        if constexpr (n == 8 * LAST_B + 8) corr_bad(IC<1>{}, IC<0>{});
        if constexpr (n == 8 * LAST_B + 9) corr_check(IC<1>{});
        if constexpr (n == 8 * LAST_B + 10 || n == 8 * LAST_B + 12) corr_mm(IC<(n - 8 * LAST_B - 10) / 2>{});
        if constexpr ((n & 1) == 1) {
            if constexpr (wc == 0) {
                sfor<issued(u) - issued(u - 1)>([&](auto k) AI { piece(IC<issued(u - 1) + decltype(k)::value>{}); });
                if constexpr (u >= T_SET && u < T_SET + 5 * SETS && (u - T_SET) % SETS == 0) att_setup(IC<(u - T_SET) / SETS>{});
                if constexpr (u == T_DMA - 1) qgo = *(volatile LAS u32*)(lds0 + L_QGO);
                if constexpr (u == T_DMA) {
                    if (__builtin_expect(__any(qgo == 0u), 0)) {
#pragma unroll 1
                        for (u32 spins = 0; spins < (SPIN_CAP << 5) && *(volatile LAS u32*)(lds0 + L_QGO) == 0u; ++spins) __builtin_amdgcn_s_sleep(1);
                    }
                    asm volatile("" ::: "memory");
                    gldsc<L_QST, 0>(ldq, l16, ws, sqs); gldsc<L_QST + 1024, 1024>(ldq, l16 + ((l16 & 512) << 1), ws, sqs); gldsc<L_QST + 2048, 3072>(ldq, l16, ws, sqs);
                    kmx = ld8c_asm<4>(ws, l16 >> 1, WS_KMAX);
                }
                if constexpr (u == T_DMA + 1) sfor<5>([&](auto ki) AI { constexpr int i = decltype(ki)::value; gldsc<L_KST + i * 1024, (i & 3) * 1024>(ldk, l16, ws, kvb + (i & 4) * 1024); });
            } else {
                if constexpr (u == T_GRAN) gldsc<L_SCR + 2048, 0>(lds0 + 2 * sst, l16, ws, sg0);
                if constexpr (u == T_GRAN + 1) glds_c(lg1, l16, ws, sg1);
                if constexpr (u == 42) gldsc<L_SCR + 2048, 0>(lds0 + 2 * sst, l16, ws, sg0 + 16 * 4096);
                if constexpr (u == P3) { if (tg == 3) __builtin_amdgcn_s_setprio(3); }
                if constexpr (u == T_POLL) { if (tg == 3) { const int ln = fresh_lane(); fl0 = ld16c_asm<2>(ws, l16 + (l16 & 896), WS_FLAGA + xcd * 4096); fl1 = ld16c_asm<2>(ws, l16 + (l16 & 896), WS_FLAGA + xcd * 4096 + 2048); fl2 = ld16c_asm<2>(ws, l16 & 48, WS_FLAGQ + tb * 256 + (hg >> 1) * 32); } }
                if constexpr (u == T_CHECK) {
                    if (tg == 3) {
                        const int ln = fresh_lane();
                        asm volatile("s_waitcnt vmcnt(0) ; APIN 2" : "+v"(fl0), "+v"(fl1), "+v"(fl2) :: "memory");
                        u32 bad = tags_bad(fl0, tag) | tags_bad(fl1, tag) | tags_bad(fl2, tag); asm volatile("" : "+v"(bad));
                        if (__builtin_expect(__any(bad != 0u), 0)) {
                            wait_flags<2>(ws, WS_FLAGA + xcd * 4096, l16 + (l16 & 896), tag);
                            wait_flags<1>(ws, WS_FLAGQ + tb * 256 + (hg >> 1) * 32, l16 & 48, tag);
                        }
                        *(volatile LAS u32*)(lds0 + L_QGO) = 1u;
                        __builtin_amdgcn_s_setprio(1);
                    }
                }
                if constexpr (u == 52) { if (tg >= 2) { const int ln = fresh_lane(); *(LAS u32x4*)(lds0 + (3 - tg) * 1024 + ln * 16) = const_rows(ln + (3 - tg) * 64); } }
            }
        }
        SBAR();
    };
    __builtin_amdgcn_s_setprio(1);
    wait_bar<wc == 0 ? 2 * (PRO_PIECES - 4) : 63>();
    asm volatile("s_waitcnt lgkmcnt(0)\n\ts_add_u32 %0, %1, 1 ; EPUSE" : "=s"(tag) : "s"(ep) : "scc");
    ntag = ~tag; asm volatile("" : "+s"(ntag));
    sfor<4>(rd_frag);
    SBAR();
    auto slot = [&](auto ni) AI { slotq(ni, IC<0>{}); };
    sfor<77>(slot);
    if (cgq < 5) sfor<8>([&](auto k) AI { slotq(IC<77 + decltype(k)::value>{}, IC<1>{}); });
    asm volatile("" : "+s"(cgq));
    if (cgq >= 5) sfor<8>([&](auto k) AI { slotq(IC<77 + decltype(k)::value>{}, IC<0>{}); });
    sfor<75>([&](auto k) AI { slot(IC<85 + decltype(k)::value>{}); });
    sfor<4>([&](auto ci) AI { epi(IC<1>{}, IC<1>{}, ci, IC<0>{}); });
    if constexpr (wc == 0) {
        __builtin_amdgcn_s_setprio(APRIO);
        const int lane = fresh_lane(), r32 = lane & 31, hh = lane >> 5;
        f32x32 o0, o1, o2, o3;
        u32x4 qa[8], kv4[6]; u32x2 qc[8], kv2[2];
        u32x4 sig = *(volatile LAS u32x4*)(lds0 + L_QSIG);
#pragma unroll
        for (int i = 0; i < 2; ++i) {
            kv4[3 * i] = *(const LAS u32x4*)((LAS char*)akv[0] + i * 2560); kv2[i] = *(const LAS u32x2*)akv[1 + i];
            kv4[3 * i + 1] = lds128(akv[3 + i]); kv4[3 * i + 2] = *(const LAS u32x4*)((LAS char*)akv[3 + i] + 640);
        }
        SBAR();
        if (__builtin_expect(__any(!(sig[0] & sig[1] & sig[2] & sig[3])), 0)) {
            u32 spins = 0;
#pragma unroll 1
            do { __builtin_amdgcn_s_sleep(1); sig = *(volatile LAS u32x4*)(lds0 + L_QSIG); } while (__any(!(sig[0] & sig[1] & sig[2] & sig[3])) && ++spins < (SPIN_CAP << 5));
        }
        asm volatile("" ::: "memory");
#pragma unroll
        for (int i = 0; i < 8; ++i) {
            qa[i] = *(const LAS u32x4*)((LAS char*)aq[i >> 1] + (i & 1) * 1536);
            qc[i] = *(const LAS u32x2*)((LAS char*)aqc[i >> 1] + (i & 1) * 1536);
        }
        SBAR();
        asm volatile("s_nop 1\nv_mov_b64 v[0:1], 0\nv_mov_b64 v[2:3], 0\nv_mov_b64 v[4:5], 0\nv_mov_b64 v[6:7], 0\nv_mov_b64 v[8:9], 0\nv_mov_b64 v[10:11], 0\nv_mov_b64 v[16:17], 0\nv_mov_b64 v[18:19], 0\nv_mov_b64 v[20:21], 0\nv_mov_b64 v[22:23], 0\nv_mov_b64 v[24:25], 0\nv_mov_b64 v[26:27], 0\nv_mov_b64 v[32:33], 0\nv_mov_b64 v[34:35], 0\nv_mov_b64 v[36:37], 0\nv_mov_b64 v[38:39], 0\nv_mov_b64 v[40:41], 0\nv_mov_b64 v[42:43], 0\nv_mov_b64 v[48:49], 0\nv_mov_b64 v[50:51], 0\nv_mov_b64 v[52:53], 0\nv_mov_b64 v[54:55], 0\nv_mov_b64 v[56:57], 0\nv_mov_b64 v[58:59], 0\nv_mov_b64 v[64:65], 0\nv_mov_b64 v[66:67], 0\nv_mov_b64 v[68:69], 0\nv_mov_b64 v[70:71], 0\nv_mov_b64 v[72:73], 0\nv_mov_b64 v[74:75], 0\nv_mov_b64 v[80:81], 0\nv_mov_b64 v[82:83], 0\nv_mov_b64 v[84:85], 0\nv_mov_b64 v[86:87], 0\nv_mov_b64 v[88:89], 0\nv_mov_b64 v[90:91], 0\nv_mov_b64 v[96:97], 0\nv_mov_b64 v[98:99], 0\nv_mov_b64 v[100:101], 0\nv_mov_b64 v[102:103], 0\nv_mov_b64 v[104:105], 0\nv_mov_b64 v[106:107], 0\nv_mov_b64 v[112:113], 0\nv_mov_b64 v[114:115], 0\nv_mov_b64 v[116:117], 0\nv_mov_b64 v[118:119], 0\nv_mov_b64 v[120:121], 0\nv_mov_b64 v[122:123], 0" : "={v[0:31]}"(o1), "={v[32:63]}"(o2), "={v[64:95]}"(o3), "={v[96:127]}"(o0));
        SBAR();
        const float k2 = wave_maxu(max(kmx[0], kmx[1])) * 1.01f;
        u32x32 q0; u32x16 q1;
        float nmax = 0.f;
#pragma unroll
        for (int i = 0; i < 8; ++i) {
            float n2 = 0.f;
#pragma unroll
            for (int e = 0; e < 4; ++e) { n2 = dot2(qa[i][e], qa[i][e], n2); q0[4 * i + e] = qa[i][e]; }
#pragma unroll
            for (int e = 0; e < 2; ++e) { n2 = dot2(qc[i][e], qc[i][e], n2); q1[2 * i + e] = qc[i][e]; }
            nmax = __uint_as_float(max(__float_as_uint(nmax), __float_as_uint(n2)));
        }
        const float bound = __builtin_amdgcn_sqrtf(x32_add(nmax) * k2);
        if (__builtin_expect(__all(bound <= 50.f), 1)) {
            const u32 nb = hh ? ((__float_as_uint(bound) >> 16) + 1) | 0x8000u : 0u;
#pragma unroll
            for (int i = 0; i < 8; ++i) q1[2 * i] |= nb;
        } else {
            float mx[8];
#pragma unroll
            for (int i = 0; i < 8; ++i) mx[i] = -INFINITY;
#pragma unroll 1
            for (int kt = 0; kt < 64; ++kt) {
                const u32x4 k0 = ld16c(ws, lane * 16, WS_KV + (par * 64 + kt) * 2560); const u32x2 k1 = ld8c(ws, hh ? 0x7ffffff0u : lane * 8, WS_KV + (par * 64 + kt) * 2560 + 1024);
#pragma unroll
                for (int i = 0; i < 8; ++i) {
                    f32x16 sc = mfma16(k0, u32x4{q0[4 * i], q0[4 * i + 1], q0[4 * i + 2], q0[4 * i + 3]}, f32x16{});
                    sc = mfma8(k1, u32x2{q1[2 * i], q1[2 * i + 1]}, sc);
#pragma unroll
                    for (int e = 0; e < 16; ++e) mx[i] = __builtin_fmaxf(mx[i], sc[e]);
                    SBAR();
                }
            }
#pragma unroll
            for (int i = 0; i < 8; ++i) { const u32x2 m3 = split3(-x32_max(mx[i])); if (hh) { q1[2 * i] = m3[0]; q1[2 * i + 1] = m3[1]; } }
        }
        u32x16 kva; u32x8 kvb8; u32x4 kvc;
        {
#pragma unroll
            for (int e = 0; e < 4; ++e) { kva[e] = kv4[0][e]; kva[4 + e] = kv4[1][e]; kva[8 + e] = kv4[2][e]; kva[12 + e] = kv4[3][e]; kvb8[e] = kv4[4][e]; kvb8[4 + e] = kv4[5][e]; }
            kvc = u32x4{kv2[0][0], kv2[0][1], kv2[1][0], kv2[1][1]};
        }
        __builtin_amdgcn_s_setprio(0);
        u32 need = 1, tmp, spin;
        asm volatile(
      "v_mov_b32 v236, 0xdddd\n"
      "s_nop 1\n"
      "v_mfma_f32_32x32x16_bf16 v[176:191], v[208:211], v[128:131], 0\n"
      "v_mfma_f32_32x32x8_bf16 v[176:191], v[232:233], v[160:161], v[176:191]\n"
      "s_nop 9\n"
      "s_waitcnt lgkmcnt(0)\n"
      "s_barrier\n"
      "v_exp_f32 v176, v176\n"
      "v_exp_f32 v177, v177\n"
      "s_setprio 2\n"
      "v_exp_f32 v178, v178\n"
      "v_exp_f32 v179, v179\n"
      "v_exp_f32 v180, v180\n"
      "v_mfma_f32_32x32x8_bf16 v[192:207], v[232:233], v[162:163], 0\n"
      "v_exp_f32 v181, v181\n"
      "v_exp_f32 v182, v182\n"
      "v_exp_f32 v183, v183\n"
      "v_exp_f32 v184, v184\n"
      "v_mfma_f32_32x32x16_bf16 v[192:207], v[208:211], v[132:135], v[192:207]\n"
      "v_exp_f32 v185, v185\n"
      "v_exp_f32 v186, v186\n"
      "v_exp_f32 v187, v187\n"
      "v_smfmac_f32_32x32x32_bf16 v[0:15], v[212:215], v[176:183], v236\n"
      "s_setprio 0\n"
      "v_exp_f32 v188, v188\n"
      "v_exp_f32 v189, v189\n"
      "v_exp_f32 v190, v190\n"
      "v_exp_f32 v191, v191\n"
      "v_exp_f32 v192, v192\n"
      "v_exp_f32 v193, v193\n"
      "v_smfmac_f32_32x32x32_bf16 v[0:15], v[216:219], v[184:191], v236\n"
      "s_setprio 2\n"
      "v_exp_f32 v194, v194\n"
      "v_exp_f32 v195, v195\n"
      "v_exp_f32 v196, v196\n"
      "v_mfma_f32_32x32x8_bf16 v[176:191], v[232:233], v[164:165], 0\n"
      "v_exp_f32 v197, v197\n"
      "v_exp_f32 v198, v198\n"
      "v_exp_f32 v199, v199\n"
      "v_exp_f32 v200, v200\n"
      "v_mfma_f32_32x32x16_bf16 v[176:191], v[208:211], v[136:139], v[176:191]\n"
      "v_exp_f32 v201, v201\n"
      "v_exp_f32 v202, v202\n"
      "v_exp_f32 v203, v203\n"
      "v_smfmac_f32_32x32x32_bf16 v[16:31], v[212:215], v[192:199], v236\n"
      "s_setprio 0\n"
      "v_exp_f32 v204, v204\n"
      "v_exp_f32 v205, v205\n"
      "v_exp_f32 v206, v206\n"
      "v_exp_f32 v207, v207\n"
      "v_exp_f32 v176, v176\n"
      "v_exp_f32 v177, v177\n"
      "v_smfmac_f32_32x32x32_bf16 v[16:31], v[216:219], v[200:207], v236\n"
      "s_setprio 2\n"
      "v_exp_f32 v178, v178\n"
      "v_exp_f32 v179, v179\n"
      "v_exp_f32 v180, v180\n"
      "v_mfma_f32_32x32x8_bf16 v[192:207], v[232:233], v[166:167], 0\n"
      "v_exp_f32 v181, v181\n"
      "v_exp_f32 v182, v182\n"
      "v_exp_f32 v183, v183\n"
      "v_exp_f32 v184, v184\n"
      "v_mfma_f32_32x32x16_bf16 v[192:207], v[208:211], v[140:143], v[192:207]\n"
      "v_exp_f32 v185, v185\n"
      "v_exp_f32 v186, v186\n"
      "v_exp_f32 v187, v187\n"
      "v_smfmac_f32_32x32x32_bf16 v[32:47], v[212:215], v[176:183], v236\n"
      "s_setprio 0\n"
      "v_exp_f32 v188, v188\n"
      "v_exp_f32 v189, v189\n"
      "v_exp_f32 v190, v190\n"
      "v_exp_f32 v191, v191\n"
      "v_exp_f32 v192, v192\n"
      "v_exp_f32 v193, v193\n"
      "v_smfmac_f32_32x32x32_bf16 v[32:47], v[216:219], v[184:191], v236\n"
      "s_setprio 2\n"
      "v_exp_f32 v194, v194\n"
      "v_exp_f32 v195, v195\n"
      "v_exp_f32 v196, v196\n"
      "v_mfma_f32_32x32x8_bf16 v[176:191], v[232:233], v[168:169], 0\n"
      "v_exp_f32 v197, v197\n"
      "v_exp_f32 v198, v198\n"
      "v_exp_f32 v199, v199\n"
      "v_exp_f32 v200, v200\n"
      "v_mfma_f32_32x32x16_bf16 v[176:191], v[208:211], v[144:147], v[176:191]\n"
      "v_exp_f32 v201, v201\n"
      "v_exp_f32 v202, v202\n"
      "v_exp_f32 v203, v203\n"
      "v_smfmac_f32_32x32x32_bf16 v[48:63], v[212:215], v[192:199], v236\n"
      "s_setprio 0\n"
      "v_exp_f32 v204, v204\n"
      "v_exp_f32 v205, v205\n"
      "v_exp_f32 v206, v206\n"
      "v_exp_f32 v207, v207\n"
      "v_exp_f32 v176, v176\n"
      "v_exp_f32 v177, v177\n"
      "v_smfmac_f32_32x32x32_bf16 v[48:63], v[216:219], v[200:207], v236\n"
      "s_setprio 2\n"
      "v_exp_f32 v178, v178\n"
      "v_exp_f32 v179, v179\n"
      "v_exp_f32 v180, v180\n"
      "v_mfma_f32_32x32x8_bf16 v[192:207], v[232:233], v[170:171], 0\n"
      "v_exp_f32 v181, v181\n"
      "v_exp_f32 v182, v182\n"
      "v_exp_f32 v183, v183\n"
      "v_exp_f32 v184, v184\n"
      "v_mfma_f32_32x32x16_bf16 v[192:207], v[208:211], v[148:151], v[192:207]\n"
      "v_exp_f32 v185, v185\n"
      "v_exp_f32 v186, v186\n"
      "v_exp_f32 v187, v187\n"
      "v_smfmac_f32_32x32x32_bf16 v[64:79], v[212:215], v[176:183], v236\n"
      "s_setprio 0\n"
      "v_exp_f32 v188, v188\n"
      "v_exp_f32 v189, v189\n"
      "v_exp_f32 v190, v190\n"
      "v_exp_f32 v191, v191\n"
      "v_exp_f32 v192, v192\n"
      "v_exp_f32 v193, v193\n"
      "v_smfmac_f32_32x32x32_bf16 v[64:79], v[216:219], v[184:191], v236\n"
      "s_setprio 2\n"
      "v_exp_f32 v194, v194\n"
      "v_exp_f32 v195, v195\n"
      "v_exp_f32 v196, v196\n"
      "v_mfma_f32_32x32x8_bf16 v[176:191], v[232:233], v[172:173], 0\n"
      "v_exp_f32 v197, v197\n"
      "v_exp_f32 v198, v198\n"
      "v_exp_f32 v199, v199\n"
      "v_exp_f32 v200, v200\n"
      "v_mfma_f32_32x32x16_bf16 v[176:191], v[208:211], v[152:155], v[176:191]\n"
      "v_exp_f32 v201, v201\n"
      "v_exp_f32 v202, v202\n"
      "v_exp_f32 v203, v203\n"
      "v_smfmac_f32_32x32x32_bf16 v[80:95], v[212:215], v[192:199], v236\n"
      "s_setprio 0\n"
      "v_exp_f32 v204, v204\n"
      "v_exp_f32 v205, v205\n"
      "v_exp_f32 v206, v206\n"
      "v_exp_f32 v207, v207\n"
      "v_exp_f32 v176, v176\n"
      "v_exp_f32 v177, v177\n"
      "v_smfmac_f32_32x32x32_bf16 v[80:95], v[216:219], v[200:207], v236\n"
      "s_setprio 2\n"
      "v_exp_f32 v178, v178\n"
      "v_exp_f32 v179, v179\n"
      "v_exp_f32 v180, v180\n"
      "v_mfma_f32_32x32x8_bf16 v[192:207], v[232:233], v[174:175], 0\n"
      "v_exp_f32 v181, v181\n"
      "v_exp_f32 v182, v182\n"
      "v_exp_f32 v183, v183\n"
      "v_exp_f32 v184, v184\n"
      "v_mfma_f32_32x32x16_bf16 v[192:207], v[208:211], v[156:159], v[192:207]\n"
      "v_exp_f32 v185, v185\n"
      "v_exp_f32 v186, v186\n"
      "v_exp_f32 v187, v187\n"
      "v_smfmac_f32_32x32x32_bf16 v[96:111], v[212:215], v[176:183], v236\n"
      "s_setprio 0\n"
      "v_exp_f32 v188, v188\n"
      "v_exp_f32 v189, v189\n"
      "v_exp_f32 v190, v190\n"
      "v_exp_f32 v191, v191\n"
      "v_exp_f32 v192, v192\n"
      "v_exp_f32 v193, v193\n"
      "v_smfmac_f32_32x32x32_bf16 v[96:111], v[216:219], v[184:191], v236\n"
      "s_setprio 2\n"
      "s_waitcnt vmcnt(0)\n"
      "v_exp_f32 v194, v194\n"
      "v_exp_f32 v195, v195\n"
      "v_exp_f32 v196, v196\n"
      "v_mfma_f32_32x32x8_bf16 v[176:191], v[234:235], v[160:161], 0\n"
      "v_exp_f32 v197, v197\n"
      "v_exp_f32 v198, v198\n"
      "v_exp_f32 v199, v199\n"
      "v_exp_f32 v200, v200\n"
      "v_mfma_f32_32x32x16_bf16 v[176:191], v[220:223], v[128:131], v[176:191]\n"
      "v_exp_f32 v201, v201\n"
      "v_exp_f32 v202, v202\n"
      "v_exp_f32 v203, v203\n"
      "v_smfmac_f32_32x32x32_bf16 v[112:127], v[212:215], v[192:199], v236\n"
      "s_setprio 0\n"
      "ds_write_b32 %[apw], v236 offset:16\n"
      "v_exp_f32 v204, v204\n"
      "v_exp_f32 v205, v205\n"
      "v_exp_f32 v206, v206\n"
      "v_exp_f32 v207, v207\n"
      "1:\n"
      "v_exp_f32 v176, v176\n"
      "v_exp_f32 v177, v177\n"
      "v_smfmac_f32_32x32x32_bf16 v[112:127], v[216:219], v[200:207], v236\n"
      "s_setprio 2\n"
      "v_exp_f32 v178, v178\n"
      "v_exp_f32 v179, v179\n"
      "v_exp_f32 v180, v180\n"
      "v_mfma_f32_32x32x8_bf16 v[192:207], v[234:235], v[162:163], 0\n"
      "v_exp_f32 v181, v181\n"
      "v_exp_f32 v182, v182\n"
      "v_exp_f32 v183, v183\n"
      "v_exp_f32 v184, v184\n"
      "v_mfma_f32_32x32x16_bf16 v[192:207], v[220:223], v[132:135], v[192:207]\n"
      "v_exp_f32 v185, v185\n"
      "v_exp_f32 v186, v186\n"
      "v_exp_f32 v187, v187\n"
      "v_smfmac_f32_32x32x32_bf16 v[0:15], v[224:227], v[176:183], v236\n"
      "s_setprio 0\n"
      "v_exp_f32 v188, v188\n"
      "v_exp_f32 v189, v189\n"
      "v_exp_f32 v190, v190\n"
      "v_exp_f32 v191, v191\n"
      "v_exp_f32 v192, v192\n"
      "v_exp_f32 v193, v193\n"
      "v_smfmac_f32_32x32x32_bf16 v[0:15], v[228:231], v[184:191], v236\n"
      "s_setprio 2\n"
      "v_exp_f32 v194, v194\n"
      "v_exp_f32 v195, v195\n"
      "v_exp_f32 v196, v196\n"
      "v_mfma_f32_32x32x8_bf16 v[176:191], v[234:235], v[164:165], 0\n"
      "v_exp_f32 v197, v197\n"
      "v_exp_f32 v198, v198\n"
      "v_exp_f32 v199, v199\n"
      "v_exp_f32 v200, v200\n"
      "v_mfma_f32_32x32x16_bf16 v[176:191], v[220:223], v[136:139], v[176:191]\n"
      "v_exp_f32 v201, v201\n"
      "v_exp_f32 v202, v202\n"
      "v_exp_f32 v203, v203\n"
      "v_smfmac_f32_32x32x32_bf16 v[16:31], v[224:227], v[192:199], v236\n"
      "s_setprio 0\n"
      "v_exp_f32 v204, v204\n"
      "v_exp_f32 v205, v205\n"
      "v_exp_f32 v206, v206\n"
      "v_exp_f32 v207, v207\n"
      "v_exp_f32 v176, v176\n"
      "v_exp_f32 v177, v177\n"
      "v_smfmac_f32_32x32x32_bf16 v[16:31], v[228:231], v[200:207], v236\n"
      "s_setprio 2\n"
      "v_exp_f32 v178, v178\n"
      "v_exp_f32 v179, v179\n"
      "v_exp_f32 v180, v180\n"
      "v_mfma_f32_32x32x8_bf16 v[192:207], v[234:235], v[166:167], 0\n"
      "v_exp_f32 v181, v181\n"
      "v_exp_f32 v182, v182\n"
      "v_exp_f32 v183, v183\n"
      "v_exp_f32 v184, v184\n"
      "v_mfma_f32_32x32x16_bf16 v[192:207], v[220:223], v[140:143], v[192:207]\n"
      "v_exp_f32 v185, v185\n"
      "v_exp_f32 v186, v186\n"
      "v_exp_f32 v187, v187\n"
      "v_smfmac_f32_32x32x32_bf16 v[32:47], v[224:227], v[176:183], v236\n"
      "s_setprio 0\n"
      "v_exp_f32 v188, v188\n"
      "v_exp_f32 v189, v189\n"
      "v_exp_f32 v190, v190\n"
      "v_exp_f32 v191, v191\n"
      "v_exp_f32 v192, v192\n"
      "v_exp_f32 v193, v193\n"
      "v_smfmac_f32_32x32x32_bf16 v[32:47], v[228:231], v[184:191], v236\n"
      "s_setprio 2\n"
      "ds_read_b32 v237, %[apw]\n"
      "v_exp_f32 v194, v194\n"
      "v_exp_f32 v195, v195\n"
      "v_exp_f32 v196, v196\n"
      "v_mfma_f32_32x32x8_bf16 v[176:191], v[234:235], v[168:169], 0\n"
      "v_exp_f32 v197, v197\n"
      "v_exp_f32 v198, v198\n"
      "v_exp_f32 v199, v199\n"
      "v_exp_f32 v200, v200\n"
      "v_mfma_f32_32x32x16_bf16 v[176:191], v[220:223], v[144:147], v[176:191]\n"
      "v_exp_f32 v201, v201\n"
      "v_exp_f32 v202, v202\n"
      "v_exp_f32 v203, v203\n"
      "v_smfmac_f32_32x32x32_bf16 v[48:63], v[224:227], v[192:199], v236\n"
      "s_setprio 0\n"
      "s_waitcnt lgkmcnt(0)\n"
      "v_exp_f32 v204, v204\n"
      "v_exp_f32 v205, v205\n"
      "v_exp_f32 v206, v206\n"
      "v_exp_f32 v207, v207\n"
      "v_exp_f32 v176, v176\n"
      "v_exp_f32 v177, v177\n"
      "v_readfirstlane_b32 %[tmp], v237\n"
      "v_smfmac_f32_32x32x32_bf16 v[48:63], v[228:231], v[200:207], v236\n"
      "s_setprio 2\n"
      "s_cmp_ge_u32 %[tmp], %[need]\n"
      "v_exp_f32 v178, v178\n"
      "v_exp_f32 v179, v179\n"
      "v_exp_f32 v180, v180\n"
      "v_mfma_f32_32x32x8_bf16 v[192:207], v[234:235], v[170:171], 0\n"
      "v_exp_f32 v181, v181\n"
      "v_exp_f32 v182, v182\n"
      "v_exp_f32 v183, v183\n"
      "v_exp_f32 v184, v184\n"
      "v_mfma_f32_32x32x16_bf16 v[192:207], v[220:223], v[148:151], v[192:207]\n"
      "v_exp_f32 v185, v185\n"
      "v_exp_f32 v186, v186\n"
      "v_exp_f32 v187, v187\n"
      "v_smfmac_f32_32x32x32_bf16 v[64:79], v[224:227], v[176:183], v236\n"
      "s_setprio 0\n"
      "s_cbranch_scc0 3f\n"
      "2:\n"
      "v_exp_f32 v188, v188\n"
      "v_exp_f32 v189, v189\n"
      "v_exp_f32 v190, v190\n"
      "v_exp_f32 v191, v191\n"
      "v_exp_f32 v192, v192\n"
      "v_exp_f32 v193, v193\n"
      "v_smfmac_f32_32x32x32_bf16 v[64:79], v[228:231], v[184:191], v236\n"
      "s_setprio 2\n"
      "ds_read_b128 v[208:211], %[ak0] offset:0\n"
      "v_exp_f32 v194, v194\n"
      "v_exp_f32 v195, v195\n"
      "v_exp_f32 v196, v196\n"
      "v_mfma_f32_32x32x8_bf16 v[176:191], v[234:235], v[172:173], 0\n"
      "v_exp_f32 v197, v197\n"
      "v_exp_f32 v198, v198\n"
      "v_exp_f32 v199, v199\n"
      "v_exp_f32 v200, v200\n"
      "v_mfma_f32_32x32x16_bf16 v[176:191], v[220:223], v[152:155], v[176:191]\n"
      "v_exp_f32 v201, v201\n"
      "v_exp_f32 v202, v202\n"
      "v_exp_f32 v203, v203\n"
      "v_smfmac_f32_32x32x32_bf16 v[80:95], v[224:227], v[192:199], v236\n"
      "s_setprio 0\n"
      "ds_read_b64 v[232:233], %[ak1] offset:0\n"
      "v_exp_f32 v204, v204\n"
      "v_exp_f32 v205, v205\n"
      "v_exp_f32 v206, v206\n"
      "v_exp_f32 v207, v207\n"
      "v_exp_f32 v176, v176\n"
      "v_exp_f32 v177, v177\n"
      "v_smfmac_f32_32x32x32_bf16 v[80:95], v[228:231], v[200:207], v236\n"
      "s_setprio 2\n"
      "ds_read_b128 v[212:215], %[av] offset:0\n"
      "v_exp_f32 v178, v178\n"
      "v_exp_f32 v179, v179\n"
      "v_exp_f32 v180, v180\n"
      "v_mfma_f32_32x32x8_bf16 v[192:207], v[234:235], v[174:175], 0\n"
      "v_exp_f32 v181, v181\n"
      "v_exp_f32 v182, v182\n"
      "v_exp_f32 v183, v183\n"
      "v_exp_f32 v184, v184\n"
      "v_mfma_f32_32x32x16_bf16 v[192:207], v[220:223], v[156:159], v[192:207]\n"
      "ds_read_b128 v[216:219], %[av] offset:640\n"
      "v_exp_f32 v185, v185\n"
      "v_exp_f32 v186, v186\n"
      "v_exp_f32 v187, v187\n"
      "v_smfmac_f32_32x32x32_bf16 v[96:111], v[224:227], v[176:183], v236\n"
      "s_setprio 0\n"
      "v_exp_f32 v188, v188\n"
      "v_exp_f32 v189, v189\n"
      "v_exp_f32 v190, v190\n"
      "v_exp_f32 v191, v191\n"
      "v_exp_f32 v192, v192\n"
      "v_exp_f32 v193, v193\n"
      "v_smfmac_f32_32x32x32_bf16 v[96:111], v[228:231], v[184:191], v236\n"
      "s_setprio 2\n"
      "s_waitcnt lgkmcnt(0)\n"
      "v_exp_f32 v194, v194\n"
      "v_exp_f32 v195, v195\n"
      "v_exp_f32 v196, v196\n"
      "v_mfma_f32_32x32x8_bf16 v[176:191], v[232:233], v[160:161], 0\n"
      "v_exp_f32 v197, v197\n"
      "v_exp_f32 v198, v198\n"
      "v_exp_f32 v199, v199\n"
      "v_exp_f32 v200, v200\n"
      "v_mfma_f32_32x32x16_bf16 v[176:191], v[208:211], v[128:131], v[176:191]\n"
      "v_exp_f32 v201, v201\n"
      "v_exp_f32 v202, v202\n"
      "v_exp_f32 v203, v203\n"
      "v_smfmac_f32_32x32x32_bf16 v[112:127], v[224:227], v[192:199], v236\n"
      "s_setprio 0\n"
      "v_exp_f32 v204, v204\n"
      "v_exp_f32 v205, v205\n"
      "v_exp_f32 v206, v206\n"
      "v_exp_f32 v207, v207\n"
      "v_exp_f32 v176, v176\n"
      "v_exp_f32 v177, v177\n"
      "v_smfmac_f32_32x32x32_bf16 v[112:127], v[228:231], v[200:207], v236\n"
      "s_setprio 2\n"
      "v_exp_f32 v178, v178\n"
      "v_exp_f32 v179, v179\n"
      "v_exp_f32 v180, v180\n"
      "v_mfma_f32_32x32x8_bf16 v[192:207], v[232:233], v[162:163], 0\n"
      "v_exp_f32 v181, v181\n"
      "v_exp_f32 v182, v182\n"
      "v_exp_f32 v183, v183\n"
      "v_exp_f32 v184, v184\n"
      "v_mfma_f32_32x32x16_bf16 v[192:207], v[208:211], v[132:135], v[192:207]\n"
      "v_exp_f32 v185, v185\n"
      "v_exp_f32 v186, v186\n"
      "v_exp_f32 v187, v187\n"
      "v_smfmac_f32_32x32x32_bf16 v[0:15], v[212:215], v[176:183], v236\n"
      "s_setprio 0\n"
      "v_exp_f32 v188, v188\n"
      "v_exp_f32 v189, v189\n"
      "v_exp_f32 v190, v190\n"
      "v_exp_f32 v191, v191\n"
      "v_exp_f32 v192, v192\n"
      "v_exp_f32 v193, v193\n"
      "v_smfmac_f32_32x32x32_bf16 v[0:15], v[216:219], v[184:191], v236\n"
      "s_setprio 2\n"
      "v_exp_f32 v194, v194\n"
      "v_exp_f32 v195, v195\n"
      "v_exp_f32 v196, v196\n"
      "v_mfma_f32_32x32x8_bf16 v[176:191], v[232:233], v[164:165], 0\n"
      "v_exp_f32 v197, v197\n"
      "v_exp_f32 v198, v198\n"
      "v_exp_f32 v199, v199\n"
      "v_exp_f32 v200, v200\n"
      "v_mfma_f32_32x32x16_bf16 v[176:191], v[208:211], v[136:139], v[176:191]\n"
      "v_exp_f32 v201, v201\n"
      "v_exp_f32 v202, v202\n"
      "v_exp_f32 v203, v203\n"
      "v_smfmac_f32_32x32x32_bf16 v[16:31], v[212:215], v[192:199], v236\n"
      "s_setprio 0\n"
      "v_exp_f32 v204, v204\n"
      "v_exp_f32 v205, v205\n"
      "v_exp_f32 v206, v206\n"
      "v_exp_f32 v207, v207\n"
      "v_exp_f32 v176, v176\n"
      "v_exp_f32 v177, v177\n"
      "v_smfmac_f32_32x32x32_bf16 v[16:31], v[216:219], v[200:207], v236\n"
      "s_setprio 2\n"
      "v_exp_f32 v178, v178\n"
      "v_exp_f32 v179, v179\n"
      "v_exp_f32 v180, v180\n"
      "v_mfma_f32_32x32x8_bf16 v[192:207], v[232:233], v[166:167], 0\n"
      "v_exp_f32 v181, v181\n"
      "v_exp_f32 v182, v182\n"
      "v_exp_f32 v183, v183\n"
      "v_exp_f32 v184, v184\n"
      "v_mfma_f32_32x32x16_bf16 v[192:207], v[208:211], v[140:143], v[192:207]\n"
      "v_exp_f32 v185, v185\n"
      "v_exp_f32 v186, v186\n"
      "v_exp_f32 v187, v187\n"
      "v_smfmac_f32_32x32x32_bf16 v[32:47], v[212:215], v[176:183], v236\n"
      "s_setprio 0\n"
      "v_exp_f32 v188, v188\n"
      "v_exp_f32 v189, v189\n"
      "v_exp_f32 v190, v190\n"
      "v_exp_f32 v191, v191\n"
      "v_exp_f32 v192, v192\n"
      "v_exp_f32 v193, v193\n"
      "v_smfmac_f32_32x32x32_bf16 v[32:47], v[216:219], v[184:191], v236\n"
      "s_setprio 2\n"
      "v_exp_f32 v194, v194\n"
      "v_exp_f32 v195, v195\n"
      "v_exp_f32 v196, v196\n"
      "v_mfma_f32_32x32x8_bf16 v[176:191], v[232:233], v[168:169], 0\n"
      "v_exp_f32 v197, v197\n"
      "v_exp_f32 v198, v198\n"
      "v_exp_f32 v199, v199\n"
      "v_exp_f32 v200, v200\n"
      "v_mfma_f32_32x32x16_bf16 v[176:191], v[208:211], v[144:147], v[176:191]\n"
      "v_exp_f32 v201, v201\n"
      "v_exp_f32 v202, v202\n"
      "v_exp_f32 v203, v203\n"
      "v_smfmac_f32_32x32x32_bf16 v[48:63], v[212:215], v[192:199], v236\n"
      "s_setprio 0\n"
      "v_exp_f32 v204, v204\n"
      "v_exp_f32 v205, v205\n"
      "v_exp_f32 v206, v206\n"
      "v_exp_f32 v207, v207\n"
      "v_exp_f32 v176, v176\n"
      "v_exp_f32 v177, v177\n"
      "v_smfmac_f32_32x32x32_bf16 v[48:63], v[216:219], v[200:207], v236\n"
      "s_setprio 2\n"
      "v_exp_f32 v178, v178\n"
      "v_exp_f32 v179, v179\n"
      "v_exp_f32 v180, v180\n"
      "v_mfma_f32_32x32x8_bf16 v[192:207], v[232:233], v[170:171], 0\n"
      "v_exp_f32 v181, v181\n"
      "v_exp_f32 v182, v182\n"
      "v_exp_f32 v183, v183\n"
      "v_exp_f32 v184, v184\n"
      "v_mfma_f32_32x32x16_bf16 v[192:207], v[208:211], v[148:151], v[192:207]\n"
      "v_exp_f32 v185, v185\n"
      "v_exp_f32 v186, v186\n"
      "v_exp_f32 v187, v187\n"
      "v_smfmac_f32_32x32x32_bf16 v[64:79], v[212:215], v[176:183], v236\n"
      "s_setprio 0\n"
      "v_exp_f32 v188, v188\n"
      "v_exp_f32 v189, v189\n"
      "v_exp_f32 v190, v190\n"
      "v_exp_f32 v191, v191\n"
      "v_exp_f32 v192, v192\n"
      "v_exp_f32 v193, v193\n"
      "v_smfmac_f32_32x32x32_bf16 v[64:79], v[216:219], v[184:191], v236\n"
      "s_setprio 2\n"
      "ds_read_b128 v[220:223], %[ak0] offset:2560\n"
      "v_exp_f32 v194, v194\n"
      "v_exp_f32 v195, v195\n"
      "v_exp_f32 v196, v196\n"
      "v_mfma_f32_32x32x8_bf16 v[176:191], v[232:233], v[172:173], 0\n"
      "v_exp_f32 v197, v197\n"
      "v_exp_f32 v198, v198\n"
      "v_exp_f32 v199, v199\n"
      "v_exp_f32 v200, v200\n"
      "v_mfma_f32_32x32x16_bf16 v[176:191], v[208:211], v[152:155], v[176:191]\n"
      "v_exp_f32 v201, v201\n"
      "v_exp_f32 v202, v202\n"
      "v_exp_f32 v203, v203\n"
      "v_smfmac_f32_32x32x32_bf16 v[80:95], v[212:215], v[192:199], v236\n"
      "s_setprio 0\n"
      "ds_read_b64 v[234:235], %[ak1] offset:2560\n"
      "v_exp_f32 v204, v204\n"
      "v_exp_f32 v205, v205\n"
      "v_exp_f32 v206, v206\n"
      "v_exp_f32 v207, v207\n"
      "v_exp_f32 v176, v176\n"
      "v_exp_f32 v177, v177\n"
      "v_smfmac_f32_32x32x32_bf16 v[80:95], v[216:219], v[200:207], v236\n"
      "s_setprio 2\n"
      "ds_read_b128 v[224:227], %[av] offset:2560\n"
      "v_exp_f32 v178, v178\n"
      "v_exp_f32 v179, v179\n"
      "v_exp_f32 v180, v180\n"
      "v_mfma_f32_32x32x8_bf16 v[192:207], v[232:233], v[174:175], 0\n"
      "v_exp_f32 v181, v181\n"
      "v_exp_f32 v182, v182\n"
      "v_exp_f32 v183, v183\n"
      "v_exp_f32 v184, v184\n"
      "v_mfma_f32_32x32x16_bf16 v[192:207], v[208:211], v[156:159], v[192:207]\n"
      "ds_read_b128 v[228:231], %[av] offset:3200\n"
      "v_exp_f32 v185, v185\n"
      "v_exp_f32 v186, v186\n"
      "v_exp_f32 v187, v187\n"
      "v_smfmac_f32_32x32x32_bf16 v[96:111], v[212:215], v[176:183], v236\n"
      "s_setprio 0\n"
      "v_exp_f32 v188, v188\n"
      "v_exp_f32 v189, v189\n"
      "v_exp_f32 v190, v190\n"
      "v_exp_f32 v191, v191\n"
      "v_exp_f32 v192, v192\n"
      "v_exp_f32 v193, v193\n"
      "v_smfmac_f32_32x32x32_bf16 v[96:111], v[216:219], v[184:191], v236\n"
      "s_setprio 2\n"
      "s_waitcnt lgkmcnt(0)\n"
      "v_exp_f32 v194, v194\n"
      "v_exp_f32 v195, v195\n"
      "v_exp_f32 v196, v196\n"
      "v_mfma_f32_32x32x8_bf16 v[176:191], v[234:235], v[160:161], 0\n"
      "v_add_u32 %[ak0], 5120, %[ak0]\n"
      "v_add_u32 %[ak1], %[sk1], %[ak1]\n"
      "v_add_u32 %[av], %[sv], %[av]\n"
      "v_exp_f32 v197, v197\n"
      "v_exp_f32 v198, v198\n"
      "v_exp_f32 v199, v199\n"
      "v_exp_f32 v200, v200\n"
      "v_mfma_f32_32x32x16_bf16 v[176:191], v[220:223], v[128:131], v[176:191]\n"
      "s_add_u32 %[need], %[need], 1\n"
      "v_exp_f32 v201, v201\n"
      "v_exp_f32 v202, v202\n"
      "v_exp_f32 v203, v203\n"
      "v_smfmac_f32_32x32x32_bf16 v[112:127], v[212:215], v[192:199], v236\n"
      "s_setprio 0\n"
      "s_cmp_lg_u32 %[need], 8\n"
      "v_exp_f32 v204, v204\n"
      "v_exp_f32 v205, v205\n"
      "v_exp_f32 v206, v206\n"
      "v_exp_f32 v207, v207\n"
      "s_cbranch_scc1 1b\n"
      ".Lfused_atail:\n"
      "v_exp_f32 v176, v176\n"
      "v_exp_f32 v177, v177\n"
      "v_smfmac_f32_32x32x32_bf16 v[112:127], v[216:219], v[200:207], v236\n"
      "s_setprio 2\n"
      "v_exp_f32 v178, v178\n"
      "v_exp_f32 v179, v179\n"
      "v_exp_f32 v180, v180\n"
      "v_mfma_f32_32x32x8_bf16 v[192:207], v[234:235], v[162:163], 0\n"
      "v_exp_f32 v181, v181\n"
      "v_exp_f32 v182, v182\n"
      "v_exp_f32 v183, v183\n"
      "v_exp_f32 v184, v184\n"
      "v_mfma_f32_32x32x16_bf16 v[192:207], v[220:223], v[132:135], v[192:207]\n"
      "v_exp_f32 v185, v185\n"
      "v_exp_f32 v186, v186\n"
      "v_exp_f32 v187, v187\n"
      "v_smfmac_f32_32x32x32_bf16 v[0:15], v[224:227], v[176:183], v236\n"
      "s_setprio 0\n"
      "v_exp_f32 v188, v188\n"
      "v_exp_f32 v189, v189\n"
      "v_exp_f32 v190, v190\n"
      "v_exp_f32 v191, v191\n"
      "v_exp_f32 v192, v192\n"
      "v_exp_f32 v193, v193\n"
      "v_smfmac_f32_32x32x32_bf16 v[0:15], v[228:231], v[184:191], v236\n"
      "s_setprio 2\n"
      "v_exp_f32 v194, v194\n"
      "v_exp_f32 v195, v195\n"
      "v_exp_f32 v196, v196\n"
      "v_mfma_f32_32x32x8_bf16 v[176:191], v[234:235], v[164:165], 0\n"
      "v_exp_f32 v197, v197\n"
      "v_exp_f32 v198, v198\n"
      "v_exp_f32 v199, v199\n"
      "v_exp_f32 v200, v200\n"
      "v_mfma_f32_32x32x16_bf16 v[176:191], v[220:223], v[136:139], v[176:191]\n"
      "ds_write_b128 %[aput], v[0:3] offset:0\n"
      "v_exp_f32 v201, v201\n"
      "v_exp_f32 v202, v202\n"
      "v_exp_f32 v203, v203\n"
      "v_smfmac_f32_32x32x32_bf16 v[16:31], v[224:227], v[192:199], v236\n"
      "s_setprio 0\n"
      "ds_write_b128 %[aput], v[4:7] offset:1024\n"
      "v_exp_f32 v204, v204\n"
      "v_exp_f32 v205, v205\n"
      "v_exp_f32 v206, v206\n"
      "v_exp_f32 v207, v207\n"
      "v_exp_f32 v176, v176\n"
      "v_exp_f32 v177, v177\n"
      "v_smfmac_f32_32x32x32_bf16 v[16:31], v[228:231], v[200:207], v236\n"
      "s_setprio 2\n"
      "ds_write_b128 %[aput], v[8:11] offset:2048\n"
      "v_exp_f32 v178, v178\n"
      "v_exp_f32 v179, v179\n"
      "v_exp_f32 v180, v180\n"
      "v_mfma_f32_32x32x8_bf16 v[192:207], v[234:235], v[166:167], 0\n"
      "v_exp_f32 v181, v181\n"
      "v_exp_f32 v182, v182\n"
      "v_exp_f32 v183, v183\n"
      "v_exp_f32 v184, v184\n"
      "v_mfma_f32_32x32x16_bf16 v[192:207], v[220:223], v[140:143], v[192:207]\n"
      "ds_write_b128 %[aput], v[16:19] offset:3072\n"
      "v_exp_f32 v185, v185\n"
      "v_exp_f32 v186, v186\n"
      "v_exp_f32 v187, v187\n"
      "v_smfmac_f32_32x32x32_bf16 v[32:47], v[224:227], v[176:183], v236\n"
      "s_setprio 0\n"
      "ds_write_b128 %[aput], v[20:23] offset:4096\n"
      "v_exp_f32 v188, v188\n"
      "v_exp_f32 v189, v189\n"
      "v_exp_f32 v190, v190\n"
      "v_exp_f32 v191, v191\n"
      "v_exp_f32 v192, v192\n"
      "v_exp_f32 v193, v193\n"
      "v_smfmac_f32_32x32x32_bf16 v[32:47], v[228:231], v[184:191], v236\n"
      "s_setprio 2\n"
      "ds_write_b128 %[aput], v[24:27] offset:5120\n"
      "v_exp_f32 v194, v194\n"
      "v_exp_f32 v195, v195\n"
      "v_exp_f32 v196, v196\n"
      "v_mfma_f32_32x32x8_bf16 v[176:191], v[234:235], v[168:169], 0\n"
      "v_exp_f32 v197, v197\n"
      "v_exp_f32 v198, v198\n"
      "v_exp_f32 v199, v199\n"
      "v_exp_f32 v200, v200\n"
      "v_mfma_f32_32x32x16_bf16 v[176:191], v[220:223], v[144:147], v[176:191]\n"
      "ds_write_b128 %[aput], v[32:35] offset:6144\n"
      "v_exp_f32 v201, v201\n"
      "v_exp_f32 v202, v202\n"
      "v_exp_f32 v203, v203\n"
      "v_smfmac_f32_32x32x32_bf16 v[48:63], v[224:227], v[192:199], v236\n"
      "s_setprio 0\n"
      "ds_write_b128 %[aput], v[36:39] offset:7168\n"
      "v_exp_f32 v204, v204\n"
      "v_exp_f32 v205, v205\n"
      "v_exp_f32 v206, v206\n"
      "v_exp_f32 v207, v207\n"
      "v_exp_f32 v176, v176\n"
      "v_exp_f32 v177, v177\n"
      "v_smfmac_f32_32x32x32_bf16 v[48:63], v[228:231], v[200:207], v236\n"
      "s_setprio 2\n"
      "ds_write_b128 %[aput], v[40:43] offset:8192\n"
      "v_exp_f32 v178, v178\n"
      "v_exp_f32 v179, v179\n"
      "v_exp_f32 v180, v180\n"
      "v_mfma_f32_32x32x8_bf16 v[192:207], v[234:235], v[170:171], 0\n"
      "v_exp_f32 v181, v181\n"
      "v_exp_f32 v182, v182\n"
      "v_exp_f32 v183, v183\n"
      "v_exp_f32 v184, v184\n"
      "v_mfma_f32_32x32x16_bf16 v[192:207], v[220:223], v[148:151], v[192:207]\n"
      "ds_write_b128 %[aput], v[48:51] offset:9216\n"
      "v_exp_f32 v185, v185\n"
      "v_exp_f32 v186, v186\n"
      "v_exp_f32 v187, v187\n"
      "v_smfmac_f32_32x32x32_bf16 v[64:79], v[224:227], v[176:183], v236\n"
      "s_setprio 0\n"
      "ds_write_b128 %[aput], v[52:55] offset:10240\n"
      "v_exp_f32 v188, v188\n"
      "v_exp_f32 v189, v189\n"
      "v_exp_f32 v190, v190\n"
      "v_exp_f32 v191, v191\n"
      "v_exp_f32 v192, v192\n"
      "v_exp_f32 v193, v193\n"
      "v_smfmac_f32_32x32x32_bf16 v[64:79], v[228:231], v[184:191], v236\n"
      "s_setprio 2\n"
      "ds_write_b128 %[aput], v[56:59] offset:11264\n"
      "v_exp_f32 v194, v194\n"
      "v_exp_f32 v195, v195\n"
      "v_exp_f32 v196, v196\n"
      "v_mfma_f32_32x32x8_bf16 v[176:191], v[234:235], v[172:173], 0\n"
      "v_exp_f32 v197, v197\n"
      "v_exp_f32 v198, v198\n"
      "v_exp_f32 v199, v199\n"
      "v_exp_f32 v200, v200\n"
      "v_mfma_f32_32x32x16_bf16 v[176:191], v[220:223], v[152:155], v[176:191]\n"
      "ds_write_b128 %[aput], v[64:67] offset:12288\n"
      "v_exp_f32 v201, v201\n"
      "v_exp_f32 v202, v202\n"
      "v_exp_f32 v203, v203\n"
      "v_smfmac_f32_32x32x32_bf16 v[80:95], v[224:227], v[192:199], v236\n"
      "s_setprio 0\n"
      "ds_write_b128 %[aput], v[68:71] offset:13312\n"
      "v_exp_f32 v204, v204\n"
      "v_exp_f32 v205, v205\n"
      "v_exp_f32 v206, v206\n"
      "v_exp_f32 v207, v207\n"
      "v_exp_f32 v176, v176\n"
      "v_exp_f32 v177, v177\n"
      "v_smfmac_f32_32x32x32_bf16 v[80:95], v[228:231], v[200:207], v236\n"
      "s_setprio 2\n"
      "ds_write_b128 %[aput], v[72:75] offset:14336\n"
      "v_exp_f32 v178, v178\n"
      "v_exp_f32 v179, v179\n"
      "v_exp_f32 v180, v180\n"
      "v_mfma_f32_32x32x8_bf16 v[192:207], v[234:235], v[174:175], 0\n"
      "v_exp_f32 v181, v181\n"
      "v_exp_f32 v182, v182\n"
      "v_exp_f32 v183, v183\n"
      "v_exp_f32 v184, v184\n"
      "v_mfma_f32_32x32x16_bf16 v[192:207], v[220:223], v[156:159], v[192:207]\n"
      "ds_write_b128 %[aput], v[80:83] offset:15360\n"
      "v_exp_f32 v185, v185\n"
      "v_exp_f32 v186, v186\n"
      "v_exp_f32 v187, v187\n"
      "v_smfmac_f32_32x32x32_bf16 v[96:111], v[224:227], v[176:183], v236\n"
      "s_setprio 0\n"
      "ds_write_b128 %[aput], v[84:87] offset:16384\n"
      "ds_write_b128 %[aput], v[88:91] offset:17408\n"
      "v_exp_f32 v188, v188\n"
      "v_exp_f32 v189, v189\n"
      "v_exp_f32 v190, v190\n"
      "v_exp_f32 v191, v191\n"
      "v_exp_f32 v192, v192\n"
      "v_exp_f32 v193, v193\n"
      "v_smfmac_f32_32x32x32_bf16 v[96:111], v[228:231], v[184:191], v236\n"
      "s_setprio 2\n"
      "s_waitcnt lgkmcnt(0)\n"
      "s_barrier\n"
      "ds_read_b128 v[0:3], %[sp0] offset:0\n"
      "ds_read_b128 v[4:7], %[sp1] offset:0\n"
      "ds_read_b128 v[8:11], %[sp2] offset:0\n"
      "ds_read_b128 v[12:15], %[sp0] offset:1024\n"
      "ds_read_b128 v[16:19], %[sp1] offset:1024\n"
      "ds_read_b128 v[20:23], %[sp2] offset:1024\n"
      "ds_read_b128 v[24:27], %[sp0] offset:2048\n"
      "ds_read_b128 v[28:31], %[sp1] offset:2048\n"
      "ds_read_b128 v[32:35], %[sp2] offset:2048\n"
      "v_exp_f32 v194, v194\n"
      "v_exp_f32 v195, v195\n"
      "v_exp_f32 v196, v196\n"
      "v_exp_f32 v197, v197\n"
      "v_exp_f32 v198, v198\n"
      "v_exp_f32 v199, v199\n"
      "v_exp_f32 v200, v200\n"
      "v_exp_f32 v201, v201\n"
      "v_exp_f32 v202, v202\n"
      "v_exp_f32 v203, v203\n"
      "v_smfmac_f32_32x32x32_bf16 v[112:127], v[224:227], v[192:199], v236\n"
      "s_setprio 0\n"
      "ds_read_b128 v[36:39], %[sp0] offset:3072\n"
      "ds_read_b128 v[40:43], %[sp1] offset:3072\n"
      "ds_read_b128 v[44:47], %[sp2] offset:3072\n"
      "ds_read_b128 v[48:51], %[sp0] offset:4096\n"
      "ds_read_b128 v[52:55], %[sp1] offset:4096\n"
      "ds_read_b128 v[56:59], %[sp2] offset:4096\n"
      "v_exp_f32 v204, v204\n"
      "v_exp_f32 v205, v205\n"
      "v_exp_f32 v206, v206\n"
      "v_exp_f32 v207, v207\n"
      "s_nop 3\n"
      "v_smfmac_f32_32x32x32_bf16 v[112:127], v[228:231], v[200:207], v236\n"
      "ds_read_b128 v[60:63], %[sp0] offset:5120\n"
      "ds_read_b128 v[64:67], %[sp1] offset:5120\n"
      "ds_read_b128 v[68:71], %[sp2] offset:5120\n"
      "s_nop 7\n"
      "s_waitcnt lgkmcnt(0)\n"
      "s_branch 4f\n"
      "3:\n"
      "s_mov_b32 %[spin], 0x400000\n"
      "5:\n"
      "s_sleep 1\n"
      "ds_read_b32 v237, %[apw]\n"
      "s_waitcnt lgkmcnt(0)\n"
      "v_readfirstlane_b32 %[tmp], v237\n"
      "s_cmp_ge_u32 %[tmp], %[need]\n"
      "s_cbranch_scc1 2b\n"
      "s_sub_u32 %[spin], %[spin], 1\n"
      "s_cmp_lg_u32 %[spin], 0\n"
      "s_cbranch_scc1 5b\n"
      "s_branch 2b\n"
      "4:\n"
            : "+{v[96:127]}"(o0), "+{v[0:31]}"(o1), "+{v[32:63]}"(o2), "+{v[64:95]}"(o3), "+{v[208:223]}"(kva), "+{v[224:231]}"(kvb8), "+{v[232:235]}"(kvc), [ak0] "+&v"(ak0), [ak1] "+&v"(ak1), [av] "+&v"(av),
              [need] "+&s"(need), [tmp] "=&s"(tmp), [spin] "=&s"(spin)
            : "{v[128:159]}"(q0), "{v[160:175]}"(q1), [sk1] "v"(sk1), [sv] "v"(sv), [aput] "v"(aput), [apw] "v"(apw), [sp0] "v"(srcp[0]), [sp1] "v"(srcp[1]), [sp2] "v"(srcp[2])
            : "v176", "v177", "v178", "v179", "v180", "v181", "v182", "v183", "v184", "v185", "v186", "v187", "v188", "v189", "v190", "v191", "v192", "v193", "v194", "v195", "v196", "v197", "v198", "v199", "v200", "v201", "v202", "v203", "v204", "v205", "v206", "v207", "v236", "v237", "scc", "memory");
        if (w == 3 && lane < 8) st4c(tag, ws, lane * 256, WS_HINT + tb * 2048 + hg * 4);
        SBAR();
        f32x4 p[3][6];
#pragma unroll
        for (int k = 0; k < 18; ++k) { const f32x32& o = k < 8 ? o1 : k < 16 ? o2 : o3; p[k % 3][k / 3] = f32x4{o[4 * (k & 7)], o[4 * (k & 7) + 1], o[4 * (k & 7) + 2], o[4 * (k & 7) + 3]}; }
        f32x4 v[6], v2[6]; float inv[2]; u32 pkd[2][6];
#pragma unroll
        for (int k = 0; k < 6; ++k) { const int g = (k + 2 + (k > 0) * 2) % 6 == 2 && k ? 4 : (k + 2 + (k > 0) * 2) % 6, o = 16 * (g / 3) + 4 * (g % 3);
            v[g] = f32x4{o0[o], o0[o + 1], o0[o + 2], o0[o + 3]} + p[0][g]; v2[g] = p[1][g] + p[2][g]; if (k == 2) { SBAR(); v[2] += v2[2]; v[5] += v2[5]; } }
        SBAR();
#pragma unroll
        for (int t = 0; t < 2; ++t) {
            auto r = __builtin_amdgcn_permlane32_swap(__float_as_uint(v[3 * t + 2][0]), __float_as_uint(v[3 * t + 2][0]), false, false);
            inv[t] = __builtin_amdgcn_rcpf(__uint_as_float(r[1]));
        }
        v[0] += v2[0]; v[1] += v2[1]; v[3] += v2[3]; v[4] += v2[4];
        SBAR();
#pragma unroll
        for (int g = 0; g < 6; ++g) v[(g + 2) % 6] *= inv[((g + 2) % 6) / 3];
        SBAR();
#pragma unroll
        for (int g = 0; g < 6; ++g) { pkd[g / 3][2 * (g % 3)] = pk2(v[g][0], v[g][1]); pkd[g / 3][2 * (g % 3) + 1] = pk2(v[g][2], v[g][3]); }
        SBAR();
#pragma unroll
        for (int k = 0; k < 12; ++k) pkd[k / 6][k % 6] = min(pkd[k / 6][k % 6], ~1u);
        o_store(vo, !hh, u32x4{pkd[0][0], pkd[0][1], pkd[0][2], pkd[0][3]}, u32x4{pkd[1][0], pkd[1][1], pkd[1][2], pkd[1][3]}, u32x4{pkd[0][4], pkd[0][5], pkd[1][4], pkd[1][5]});
        asm volatile("s_barrier" ::: "memory");
        } else {
        __builtin_amdgcn_s_setprio(DPRIO);
        const int lane = fresh_lane();
        st16c(hold[0][0], ws, vep1, 0); st16c(hold[0][1], ws, vep1, 512); st16c(hold[1][0], ws, vep, 0); st16c(hold[1][1], ws, vep, 512);
        wait_bar<63>();
        __builtin_amdgcn_s_setprio(SPRIO);
        u32 deadline = (u32)__builtin_amdgcn_s_memtime() + T_ATT - 12 * T_STEP;
        {
            const u32 dst = lds0 + L_ATT + d * L_AW, src = kvb + 5120;
            auto pair = [&](auto ji) AI { constexpr int jj = decltype(ji)::value; sfor<5>([&](auto k) AI { constexpr int i = decltype(k)::value; gldsc<jj * 5120 + i * 1024, (i & 3) * 1024>(dst, l16, ws, src + jj * 5120 + (i & 4) * 1024); }); };
            auto prog = [&](u32 v) AI { *(volatile LAS u32*)(lds0 + L_PROG + d * 4) = v; };
            pair(IC<0>{});
            sfor<5>([&](auto k) AI { constexpr int it = decltype(k)::value; glds<6 * 5120 + it * 1024, it * 128>(dst, vwl, rwt, wtso); });
            pair(IC<1>{});
            *(LAS u32x4*)(pl + 2560) = cvr;
            wait_vm<HPOS == 2 ? 14 : 10>(); prog(1);
            wait_vm<HPOS == 2 ? 9 : 5>();
            {
#pragma unroll
                for (int it = 0; it < 5; ++it) {
                    const u32x2 lo = lds_trp((LAS char*)awt + it * 1024), hi = lds_trp((LAS char*)awt + it * 1024 + 512);
                    st16c(u32x4{lo[0], lo[1], hi[0], hi[1]}, ws, vws[it], 0);
                }
            }
            pair(IC<2>{});
            wait_vm<5>(); prog(2);
            if (tg == 3) {
#pragma unroll 1
                for (u32 spins = 0; spins < (SPIN_CAP << 4) && !__all(*(volatile LAS u32*)(lds0 + (lane & 7) * 4) > 1u); ++spins) __builtin_amdgcn_s_sleep(1);
                if (lane < 8) st8c(u32x2{tag, ntag}, ws, lane * 4096, WS_FLAGW + (b >> 4) * 256 + (b & 15) * 8);
            }
            pair(IC<3>{}); pair(IC<4>{}); pair(IC<5>{}); pair(IC<6>{});
            st16c(unw, ws, vso, 0); st16c(unw, ws, vso, 512); st16c(unw, ws, vso2, 0);
            wait_vm<18>(); prog(4);
            sfor<5>([&](auto ci) AI { constexpr int c = decltype(ci)::value; xv[c] = ld8_asm<13>(rsx, vxr, ctile(c) * 32); });
            if (tg == 0) { __builtin_amdgcn_s_sleep(WDELAY); fl0 = ld16c_asm<14>(ws, l16 + (l16 & 896), WS_FLAGW + xcd * 4096); fl1 = ld16c_asm<14>(ws, l16 + (l16 & 896), WS_FLAGW + xcd * 4096 + 2048); }
            asm volatile("s_waitcnt vmcnt(0) ; APIN 14" : "+v"(fl0), "+v"(fl1) :: "memory");
            prog(7);
            asm volatile("; APIN 13" : "+v"(xv[0]), "+v"(xv[1]), "+v"(xv[2]), "+v"(xv[3]), "+v"(xv[4]));
#pragma unroll
            for (int c = 0; c < 5; ++c) {
                acc2[c][0] = f32x4{bflo(xv[c][0]), bfhi(xv[c][0]), bflo(xv[c][1]), bfhi(xv[c][1])};
                asm volatile("" : "+v"(acc2[c][0]));
            }
            if (lane == 0) st8c(u32x2{tag, ntag}, ws, 0, WS_FLAGC + tb * 256 + (hg * 4 + tg) * 8);
        }
        if (tg == 0) {
            if (__any((tags_bad(fl0, tag) | tags_bad(fl1, tag)) != 0u)) wait_flags<2, WSLEEP>(ws, WS_FLAGW + xcd * 4096, l16 + (l16 & 896), tag);
            *(volatile LAS u32*)(lds0 + L_GO) = 1u;
        }
        else {
#pragma unroll 1
            for (u32 spins = 0; spins < (SPIN_CAP << 4) && *(volatile LAS u32*)(lds0 + L_GO) == 0u; ++spins) __builtin_amdgcn_s_sleep(2);
            asm volatile("" ::: "memory");
        }
        const u32 va = l16;
        u32 sA = WS_WT + (hg * 5 + par) * TS, sA4 = WS_WT + (hg * 5 + ctile(4)) * TS; const u32 sB = WS_ACT + tb * 4 * TS;
        asm volatile("" : "+s"(sA), "+s"(sA4));
        auto sAc = [&](int c) AI { return c < 4 ? sA + c * TS : sA4; };
        u32x4 fa[3][5], fb[5][4];
        auto lda = [&](int slot, int S) AI {
#pragma unroll
            for (int c = 0; c < 5; ++c) fa[slot][c] = ld16c(ws, va, sAc(c) + S * 1024);
        };
        auto ldb = [&](int slot, int S) AI {
#pragma unroll
            for (int t = 0; t < 4; ++t) fb[slot][t] = ld16c(ws, vb[t], sB + S * 1024);
        };
        auto stepm = [&](auto ci, int nxt, int S) AI {
            constexpr int cur = decltype(ci)::value;
            int k = 0, e = 0;
#pragma unroll
            for (int m = 0; m < 20; ++m) {
                acc2[m >> 2][m & 3] = mfma_s(fa[cur][m >> 2], fb[cur][m & 3], acc2[m >> 2][m & 3]);
                SBAR();
                if (group_end(cur * 20 + m)) {
#pragma unroll
                    for (int r = 0; r < 3; ++r) if (k < 9 && (r == 0 || (r == 1 && (GPAT > 1 || e < 3)) || (r == 2 && GPAT == 5 && e == 0))) { if (k < 4) fb[nxt][k] = ld16c(ws, vb[k], sB + S * 1024); else fa[nxt][k - 4] = ld16c(ws, va, sAc(k - 4) + S * 1024); ++k; }
                    SBAR();
                    if (e == 0) {
                        u64 now;
                        asm volatile("s_memtime %0\n\ts_waitcnt lgkmcnt(0)" : "=s"(now));
                        asm volatile("s_setprio 1\n\ts_cmp_gt_i32 %0, 0\n\ts_cbranch_scc0 1f\n\ts_setprio 3\n1:" :: "s"((u32)now - deadline) : "scc");
                        deadline += T_STEP;
                    } else asm volatile("s_nop %c0" :: "i"(PACE));
                    SBAR();
                    ++e;
                }
            }
        };
        int So[5];
#pragma unroll
        for (int i = 0; i < 5; ++i) { const int r = i + 1 + ((5 * hg + 2) >> 3); So[i] = 54 + d + 4 * (r >= 5 ? r - 5 : r); }
        u32x4 fo[5][5];
        auto ldo_w = [&](int i) AI {
#pragma unroll
            for (int c = 0; c < 5; ++c) fo[i][c] = ld16c(ws, va, sAc(c) + So[i] * 1024);
        };
        ldb(0, d); lda(0, d); ldb(1, d + 4); lda(1, d + 4);
        SBAR(); __builtin_amdgcn_s_setprio(1); SBAR();
#pragma unroll 1
        for (int i = 0; i < NDOWN; i += 3) { stepm(IC<0>{}, 2, d + 4 * (i + 2)); stepm(IC<1>{}, 0, d + 4 * (i + 3)); stepm(IC<2>{}, 1, min(d + 4 * (i + 4), 52 + (d & 1))); }
        u32 dm;
        {
            u64 here; u32 back, fwd;
            asm volatile("s_getpc_b64 %0\n\ts_mov_b32 %1, .-.Lfused_atail\n\ts_mov_b32 %2, .Lfused_last-." : "=s"(here), "=s"(back), "=s"(fwd));
            asm volatile("global_load_dword %0, %1, %2 ; ALD 15" : "=v"(dm) : "v"(tg == 0 ? min((u32)l16 * 8, fwd) + back : (u32)(l16 & 511) * 8), "s"(here - back) : "memory");
        }
        const u32x4 flc = ld16c(ws, l16 & 255, WS_FLAGC + tb * 256);
#pragma unroll
        for (int m = 0; m < 10; ++m) {
            acc2[m >> 1][m & 1] = mfma_s(fa[1][m >> 1], fb[1][m & 1], acc2[m >> 1][m & 1]);
            SBAR();
            if (m == 2 || m == 5) { ldo_w(m / 3); SBAR(); }
        }
        ldo_w(2); ldo_w(3);
        ldo_w(4);
        const u32 own = lds0 + L_ATT + d * L_AW + L_FINO + lane * 16;
        wait_bar<63>();
        __builtin_amdgcn_s_setprio(0);
        if (__builtin_expect(!__all(tags_ok(flc, tag)), 0)) wait_flags<1>(ws, WS_FLAGC + tb * 256, l16 & 255, tag);
        asm volatile("; APIN 15" : "+v"(dm));
        u32 src[3];
#pragma unroll
        for (int t = 1; t < 4; ++t) src[t - 1] = lds0 + L_ATT + ((d - t) & 3) * L_AW + L_FINO + (t - 1) * 5120 + lane * 16;
        const u32 vrow = (tb * 64 + d * 16 + ((l16 >> 4) & 15)) * 1280 + hg * 160 + ((l16 >> 5) & 16);
        u32 vout2 = vrow + par * 32 + ((l16 >> 3) & 32), vout4 = (l16 & 256) ? 0x7f000000u : vrow + ctile(4) * 32;
        asm volatile("" : "+v"(src[0]), "+v"(src[1]), "+v"(src[2]), "+v"(vout2), "+v"(vout4));
        asm volatile("s_barrier" ::: "memory");
        __builtin_amdgcn_s_sleep(DELAY0);
        u32 hint = ld4c(ws, (l16 >> 2) & 28, WS_HINT + (tb * 8 + hg) * 256);
        SBAR();
#pragma unroll
        for (int m = 0; m < 20; ++m) acc2[m >> 2][m & 3] = mfma_s(fa[0][m >> 2], fb[0][m & 3], acc2[m >> 2][m & 3]);
        SBAR();
        if (!__all(hint == tag)) {
            u32 spins = 0;
#pragma unroll 1
            do {
                __builtin_amdgcn_s_sleep(HSLEEP);
                asm volatile("" ::: "memory");
                hint = ld4c(ws, (l16 >> 2) & 28, WS_HINT + (tb * 8 + hg) * 256);
            } while (!__all(hint == tag) && ++spins < SPIN_CAP);
            __builtin_amdgcn_s_sleep(HLATE);
        }
        __builtin_amdgcn_s_sleep(HINT_EARLY);
        {
            auto ldo = [&](auto ii, int t) AI { constexpr int i = decltype(ii)::value; fb[i][t] = ld16c_asm<6 + i>(ws, vb[t], sB + So[i] * 1024); };
            auto missing = [&](int i) AI { u32 m = 0;
#pragma unroll
                for (int t = 0; t < 4; ++t) m = max(max(m, max(fb[i][t][0], fb[i][t][1])), max(fb[i][t][2], fb[i][t][3]));
                return m == ~0u; };
            bool bad;
            auto arrive = [&](auto ii, auto ni) AI {
                constexpr int i = decltype(ii)::value;
                asm volatile("s_waitcnt vmcnt(%c5) ; APIN %c4" : "+v"(fb[i][0]), "+v"(fb[i][1]), "+v"(fb[i][2]), "+v"(fb[i][3]) : "i"(6 + i), "i"(decltype(ni)::value) : "memory");
                bad = missing(i);
            };
            sfor<3>([&](auto ii) AI {
#pragma unroll
                for (int t = 0; t < 4; ++t) ldo(ii, t);
            });
            arrive(IC<0>{}, IC<8>{});
            sfor<5>([&](auto ii) AI {
                constexpr int i = decltype(ii)::value;
                if (__builtin_expect(__any(bad), 0)) {
                    u32 spins = 0;
#pragma unroll 1
                    do {
                        asm volatile("" ::: "memory");
#pragma unroll
                        for (int t = 0; t < 4; ++t) fb[i][t] = ld16c(ws, vb[t], sB + So[i] * 1024);
                    } while (__any(missing(i)) && ++spins < SPIN_CAP);
                }
                SBAR();
#pragma unroll
                for (int t0 = 0; t0 < 4; ++t0) {
                    const int t = i < 4 ? t0 : (t0 + 1) & 3;
                    if constexpr (i < 4) { if (t == 3) arrive(IC<i + 1>{}, IC<(i < 2 ? 7 : i == 2 ? 4 : 0)>{}); }
#pragma unroll
                    for (int c = 0; c < 5; ++c) {
                        acc2[c][t] = mfma_s(fo[i][c], fb[i][t], acc2[c][t]);
                        if (i == 4 && t0 > 0) { SBAR(); *(LAS f32x4*)(own + ((t0 - 1) * 5 + c) * 1024) = acc2[c][t0]; SBAR(); }
                    }
                    SBAR();
                    if constexpr (i < 2) ldo(IC<i + 3>{}, t);
                    SBAR();
                }
            });
        }
        wait_bar<63>();
        f32x4 p[3][5];
#pragma unroll
        for (int c = 0; c < 5; ++c)
#pragma unroll
            for (int t = 0; t < 3; ++t) p[t][c] = *(const LAS f32x4*)((LAS char*)src[t] + c * 1024);
        SBAR();
#pragma unroll
        for (int c = 0; c < 5; ++c) { acc2[c][0] += p[0][c]; p[1][c] += p[2][c]; }
        SBAR();
#pragma unroll
        for (int c = 0; c < 5; ++c) acc2[c][0] += p[1][c];
        SBAR();
        u32 pk[5][2];
#pragma unroll
        for (int c = 0; c < 5; ++c) { pk[c][0] = pk2(acc2[c][0][0], acc2[c][0][1]); pk[c][1] = pk2(acc2[c][0][2], acc2[c][0][3]); }
        const rsrc_t ro = mkrs(out, S_ * D_ * 2);
#pragma unroll
        for (int c = 0; c < 5; c += 2) {
            const auto r0 = __builtin_amdgcn_permlane16_swap(pk[c][0], pk[min(c + 1, 4)][0], false, false), r1 = __builtin_amdgcn_permlane16_swap(pk[c][1], pk[min(c + 1, 4)][1], false, false);
            __builtin_amdgcn_raw_buffer_store_b128(u32x4{r0[0], r1[0], r0[1], r1[1]}, ro, c < 4 ? vout2 + c * 32 : vout4, 0, OUTAUX);
        }
        asm volatile(".Lfused_last:");
        if (b == 0 && w == 4 && lane == 0) st4c(tag, ws, 0, WS_EPOCH);
        }
}


__global__ __launch_bounds__(512, 2) void fused_kernel(const u16* __restrict__ x, const u16* __restrict__ wq, const u16* __restrict__ wk, const u16* __restrict__ wv,
                                                       const u16* __restrict__ wo, const u16* __restrict__ wg, const u16* __restrict__ wu, const u16* __restrict__ wd,
                                                       u16* __restrict__ out, char* __restrict__ wsp) {
    __shared__ __attribute__((aligned(1024))) char smem[L_TOTAL];
    const u32 lds0 = (u32)(uintptr_t)(LAS char*)smem;
    const int w = __builtin_amdgcn_readfirstlane(threadIdx.x >> 6);
    u64 pc; u32 code_bytes; asm volatile("s_getpc_b64 %0\n\ts_mov_b32 %1, .Lfused_last-." : "=s"(pc), "=s"(code_bytes));
    int role = w; asm volatile("" : "+s"(role));
    if (role < 4) program<0>(x, wq, wk, wv, wo, wg, wu, wd, out, wsp, lds0, w, pc, code_bytes);
    asm volatile("" : "+s"(role));
    if (role >= 4) program<1>(x, wq, wk, wv, wo, wg, wu, wd, out, wsp, lds0, w, pc, code_bytes);
}

extern "C" void kernel_launch(void* const* d_in, const int* in_sizes, int n_in, void* d_out, int out_size, void* d_ws, size_t ws_size, hipStream_t stream) {
    const u16 *x = (const u16*)d_in[0], *wq = (const u16*)d_in[1], *wk = (const u16*)d_in[2], *wv = (const u16*)d_in[3], *wo = (const u16*)d_in[4];
    const u16 *wg = (const u16*)d_in[6], *wu = (const u16*)d_in[7], *wd = (const u16*)d_in[8];
    fused_kernel<<<256, 512, 0, stream>>>(x, wq, wk, wv, wo, wg, wu, wd, (u16*)d_out, (char*)d_ws);
}
```
